```python
import torch

def _softmax(x, axis=-1):
    return torch.softmax(x, dim=axis)
def _gelu(x):
    return torch.nn.functional.gelu(x, approximate="tanh")
def _silu(x):
    return torch.nn.functional.silu(x)
def _sigmoid(x):
    return torch.sigmoid(x)
def _layernorm(x, axis=-1, eps=1e-5):
    m = x.mean(dim=axis, keepdim=True)
    v = x.var(dim=axis, keepdim=True, unbiased=False)
    return (x - m) / torch.sqrt(v + eps)
def _scatter_add(tgt, idx, src, axis=0):
    return tgt.clone().index_add_(axis, idx, src)
def _segment_sum(src, seg, n):
    out = torch.zeros((n,) + src.shape[1:], dtype=src.dtype)
    return out.index_add_(0, seg, src)
def _pad(x, pw):
    flat = tuple(v for pair in reversed(pw) for v in pair)
    return torch.nn.functional.pad(x, flat)

kernel_name = 'interleave_layers__mha_block_cached__glu_gated_glu__decode'

def setup_inputs():

    _w = (lambda a: (lambda t: t.to(torch.bfloat16) if t.is_floating_point() else t)(torch.as_tensor(a)))
    def _A_setup():
        B, Sq, D = 2, 1, 448
        H, Sk, Dh = 16, 16384, 256
        return {
            "x": _w((lambda *s: torch.randn(*s, dtype=torch.bfloat16) * 0.05)(B, Sq, D)),
            "k_cache": _w((lambda *s: torch.randn(*s, dtype=torch.bfloat16) * 1.6)(B, H, Sk, Dh)),
            "v_cache": _w((lambda *s: torch.randn(*s, dtype=torch.bfloat16) * 1.6)(B, H, Sk, Dh)),
            "wq": _w((lambda *s: torch.randn(*s, dtype=torch.bfloat16) * 1.6)(D, H * Dh) * (D ** -0.5)),
            "wo": _w((lambda *s: torch.randn(*s, dtype=torch.bfloat16) * 1.6)(H * Dh, D) * ((H * Dh) ** -0.5)),
        }
    _A_in = _A_setup()

    def _B_setup():
        B, S, D, F = 2, 1, 448, 1792
        return {
            "x": _w((lambda *s: torch.randn(*s, dtype=torch.bfloat16) * 0.05)(B, S, D)),
            "w_gate": _w((lambda *s: torch.randn(*s, dtype=torch.bfloat16) * 0.05)(D, F)),
            "w_up": _w((lambda *s: torch.randn(*s, dtype=torch.bfloat16) * 0.05)(D, F)),
            "w_down": _w((lambda *s: torch.randn(*s, dtype=torch.bfloat16) * 0.05)(F, D)),
        }
    _B_in = _B_setup()

    return {
        "A_x": _A_in["x"],
        "A_k_cache": _A_in["k_cache"],
        "A_v_cache": _A_in["v_cache"],
        "A_wq": _A_in["wq"],
        "A_wo": _A_in["wo"],
        "B_x": _B_in["x"],
        "B_w_gate": _B_in["w_gate"],
        "B_w_up": _B_in["w_up"],
        "B_w_down": _B_in["w_down"],
    }

def reference(A_x, A_k_cache, A_v_cache, A_wq, A_wo, B_x, B_w_gate, B_w_up, B_w_down):
    def _A_ref(x, k_cache, v_cache, wq, wo):
        B, Sq, D = x.shape
        H, Dh = 16, 256
        q = (lambda x, *d: x.permute(*d))((x @ wq).reshape(B, Sq, H, Dh), 0, 2, 1, 3)
        scores = torch.einsum("bhqd,bhkd->bhqk", q, k_cache) * (Dh ** -0.5)
        p = _softmax(scores, axis=-1)
        o = torch.einsum("bhqk,bhkd->bhqd", p, v_cache)
        return (lambda x, *d: x.permute(*d))(o, 0, 2, 1, 3).reshape(B, Sq, H * Dh) @ wo

    def _B_ref(x, w_gate, w_up, w_down):
        g = torch.einsum("bsd,df->bsf", x, w_gate)
        u = torch.einsum("bsd,df->bsf", x, w_up)
        h = _sigmoid(g) * u
        return torch.einsum("bsf,fd->bsd", h, w_down)

    x = A_x
    x = x + _A_ref(_layernorm(x, axis=-1), A_k_cache, A_v_cache, A_wq, A_wo)
    x = x + _B_ref(_layernorm(x, axis=-1), B_w_gate, B_w_up, B_w_down)
    x = x + _A_ref(_layernorm(x, axis=-1), A_k_cache, A_v_cache, A_wq, A_wo)
    x = x + _B_ref(_layernorm(x, axis=-1), B_w_gate, B_w_up, B_w_down)
    return x
```

```cpp
#include <hip/hip_runtime.h>
#include <hip/hip_bf16.h>

typedef __attribute__((ext_vector_type(4))) float f32x4;
typedef __attribute__((ext_vector_type(4))) unsigned u32x4;
typedef __attribute__((ext_vector_type(2))) unsigned u32x2;

constexpr int D = 448, FF = 1792, NATT = 240, NBLK = 137;
constexpr int WS_Q = 0;
constexpr int WS_Y = 65536, WS_Z = WS_Y + 16 * 2048, WS_F = WS_Z + 16 * 2048, WS_E = WS_F + 256;
constexpr int WS_P = 262144;
constexpr int WS_END = WS_P + 2 * 240 * 2080;
#define PA 10
#define PB 20
#define PC 46
constexpr int SPIN_CAP = 200000;

__device__ __forceinline__ float bfr(float x) { return __bfloat162float(__float2bfloat16(x)); }
__device__ __forceinline__ unsigned short bfbits(float x) { return __builtin_bit_cast(unsigned short, __float2bfloat16(x)); }
__device__ __forceinline__ float bf2f(unsigned short b) { return __uint_as_float((unsigned)b << 16); }
typedef __amdgpu_buffer_rsrc_t rsrc_t;
__device__ __forceinline__ u32x2 gload(rsrc_t r, int voff, int soff) { return __builtin_amdgcn_raw_buffer_load_b64(r, voff, soff, 16); }
__device__ __forceinline__ void gstore(rsrc_t r, int voff, int soff, unsigned v, unsigned tag) {
  u32x2 d; d[0] = v; d[1] = tag;
  __builtin_amdgcn_raw_buffer_store_b64(d, r, voff, soff, 16);
}
#define MFENCE() asm volatile("" ::: "memory")
template <int CTRL> __device__ __forceinline__ float dppf(float x) {
  return __builtin_bit_cast(float, __builtin_amdgcn_mov_dpp(__builtin_bit_cast(int, x), CTRL, 0xf, 0xf, true));
}
__device__ __forceinline__ float wsum(float x) {
  x += dppf<0xB1>(x); x += dppf<0x4E>(x); x += dppf<0x141>(x); x += dppf<0x140>(x);
  auto s = __builtin_amdgcn_permlane16_swap(__float_as_uint(x), __float_as_uint(x), false, false);
  x = __uint_as_float(s[0]) + __uint_as_float(s[1]);
  auto t = __builtin_amdgcn_permlane32_swap(__float_as_uint(x), __float_as_uint(x), false, false);
  return __uint_as_float(t[0]) + __uint_as_float(t[1]);
}
__device__ __forceinline__ void dma16(const char* g, char* l) {
  __builtin_amdgcn_global_load_lds((const __attribute__((address_space(1))) void*)g, (__attribute__((address_space(3))) void*)l, 16, 0, 2);
}
#define TSC(i)
#define LFENCE() do { asm volatile("s_waitcnt lgkmcnt(0)" ::: "memory"); __builtin_amdgcn_sched_barrier(0); } while (0)
#define VFENCE() do { asm volatile("s_waitcnt vmcnt(0)" ::: "memory"); __builtin_amdgcn_sched_barrier(0); } while (0)

constexpr int C_WD = 0;
constexpr int C_GU = 100352;
constexpr int C_RED = C_GU + 36864;
constexpr int C_VEC = C_RED + 14336;
constexpr int C_X = C_VEC + 4 * 2176;

__device__ __forceinline__ void mma(int n, u32x4 b, f32x4& c) {
  asm volatile("s_nop 1\nv_mfma_f32_16x16x32_bf16 %0, a[%1:%2], %3, %0" : "+v"(c) : "i"(4 * n), "i"(4 * n + 3), "v"(b));
}
template <bool NT> __device__ __forceinline__ void aload(int n, u32x4 r, int voff, int soff, int imm) {
  if (NT) asm volatile("s_nop 4\nbuffer_load_dwordx4 a[%0:%1], %2, %3, %4 offen offset:%5 nt" ::"i"(4 * n), "i"(4 * n + 3), "v"(voff), "s"(r), "s"(soff), "i"(imm) : "memory");
  else asm volatile("s_nop 4\nbuffer_load_dwordx4 a[%0:%1], %2, %3, %4 offen offset:%5" ::"i"(4 * n), "i"(4 * n + 3), "v"(voff), "s"(r), "s"(soff), "i"(imm) : "memory");
}
__device__ __forceinline__ u32x4 mkdesc(const void* p, unsigned n) {
  u32x4 r; r[0] = (unsigned)(size_t)p; r[1] = (unsigned)((size_t)p >> 32); r[2] = n; r[3] = 0x20000; return r;
}
__device__ __forceinline__ void mmv(u32x4 a, u32x4 b, f32x4& c) { asm volatile("s_nop 1\nv_mfma_f32_16x16x32_bf16 %0, %1, %2, %0" : "+v"(c) : "v"(a), "v"(b)); }
__device__ __forceinline__ u32x4 bop(u32x4 msk, unsigned x) { return msk & x; }
template <int N> __device__ __forceinline__ void mm_begin(f32x4 (&c)[N]) {
#pragma unroll
  for (int i = 0; i < N; i++) { c[i] = f32x4{}; asm volatile("" : "+v"(c[i])); }
}
template <int N> __device__ __forceinline__ void mm_end(f32x4 (&c)[N]) {
  asm volatile("s_nop 7\ns_nop 7" ::: "memory");
#pragma unroll
  for (int i = 0; i < N; i++) asm volatile("" : "+v"(c[i]));
}
__device__ __forceinline__ u32x4 mkmask(int lane) {
  u32x4 m;
#pragma unroll
  for (int k = 0; k < 4; k++) m[k] = ((lane & 7) >> 1) == k ? ((lane & 1) ? 0xffff0000u : 0xffffu) : 0u;
  return m;
}
__device__ __forceinline__ u32x4 wload(rsrc_t r, int voff, int soff) { return __builtin_amdgcn_raw_buffer_load_b128(r, voff, soff, 2); }
__device__ __forceinline__ unsigned dup(float x) { return (unsigned)bfbits(x) * 0x10001u; }
__device__ __forceinline__ void put2(float* red, f32x4 a0, f32x4 a1, int lane) {
#pragma unroll
  for (int i = 0; i < 4; i++) {
    float v = (lane & 8) ? a1[i] : a0[i];
    v += dppf<0x128>(v);
    red[32 * (lane >> 4) + 8 * i + (lane & 7)] = v;
  }
}
__device__ __forceinline__ void put448(float* red, const f32x4 (&acc)[7], int lane) {
#pragma unroll
  for (int s = 0; s < 3; s++) put2(red + 128 * s, acc[s], acc[3 + s], lane);
#pragma unroll
  for (int i = 0; i < 4; i++) {
    float v = ((lane >> 5) == ((lane >> 3) & 1)) ? acc[6][i] : 0.f;
    v += dppf<0x128>(v);
    auto p = __builtin_amdgcn_permlane32_swap(__float_as_uint(v), __float_as_uint(v), false, false);
    v = __uint_as_float(p[0]) + __uint_as_float(p[1]);
    red[384 + 2 * (lane & 16) + 8 * i + (lane & 7)] = v;
  }
}

__device__ __forceinline__ void ln_wave(const float (&x)[7], unsigned* vec, int lane) {
  float s = 0.f;
#pragma unroll
  for (int i = 0; i < 7; i++) s += x[i];
  const float mean = wsum(s) * (1.0f / D);
  float v = 0.f;
#pragma unroll
  for (int i = 0; i < 7; i++) { const float d = x[i] - mean; v += d * d; }
  const float var = bfr(wsum(v) * (1.0f / D));
  const float mb = bfr(mean), rs = __builtin_amdgcn_rcpf(bfr(__builtin_amdgcn_sqrtf(bfr(var + 1e-5f))));
#pragma unroll
  for (int i = 0; i < 7; i++) vec[(lane & 7) * 68 + (lane >> 3) + 8 * i] = dup(bfr(x[i] - mb) * rs);
  LFENCE();
}
template <int I0, int I1, bool NT> __device__ __forceinline__ void load_wq(u32x4 r, int c, int w, int lane) {
  const int voff = (lane >> 4) * 16384 + (lane & 15) * 16;
#pragma unroll
  for (int i = I0; i < I1; i++) aload<NT>(i, r, voff, (112 * w + 8 * (i >> 2) + ((i >> 1) & 1)) * 8192 + c * 512, (i & 1) * 256);
}
__device__ __forceinline__ void load_wo(u32x4 r, int c, int w, int lane) {
  const int voff = (lane >> 4) * 1792 + (lane & 15) * 16, vrem = (lane >> 3) * 896 + 768 + (lane & 7) * 16;
#pragma unroll
  for (int t = 0; t < 8; t++) {
    const int so = (c * 256 + 128 * (t >> 2) + 32 * w + 8 * (t & 3)) * 896;
#pragma unroll
    for (int i = 0; i < 6; i++) aload<true>(t * 7 + i, r, voff, so, (i / 3) * 896 + (i % 3) * 256);
    aload<true>(t * 7 + 6, r, vrem, so, 0);
  }
}
__device__ __forceinline__ u32x4 down_tile(const char* wd, int n, int w, int lane) {
  const int i = n % 7;
  const char* base = wd + min(w + 4 * (n / 7), 13) * 7168;
  return *(const u32x4*)(base + (i < 6 ? (lane >> 4) * 1792 + (lane & 15) * 16 + (i / 3) * 896 + (i % 3) * 256 : (lane >> 3) * 896 + 768 + (lane & 7) * 16));
}
__device__ __forceinline__ void q_proj(const unsigned* vr, u32x4 msk, float* red, rsrc_t ws, int qdst, unsigned tag, int tid, int w) {
  f32x4 acc[4];
  unsigned xv[14];
#pragma unroll
  for (int t = 0; t < 14; t++) xv[t] = vr[14 * w + t];
  mm_begin(acc);
#pragma unroll
  for (int t = 0; t < 14; t++) {
    const u32x4 b = bop(msk, xv[t]);
    if (t == PC / 4) VFENCE();
#pragma unroll
    for (int i = 0; i < 4; i++) mma(t * 4 + i, b, acc[i]);
  }
  mm_end(acc);
  put2(red + w * 256, acc[0], acc[2], tid & 63);
  put2(red + w * 256 + 128, acc[1], acc[3], tid & 63);
  __syncthreads();
  if (tid < 128) {
    float a = 0.f, b = 0.f;
#pragma unroll
    for (int g = 0; g < 4; g++) { a += red[g * 256 + 2 * tid]; b += red[g * 256 + 2 * tid + 1]; }
    gstore(ws, tid * 8, qdst, (unsigned)bfbits(a) | ((unsigned)bfbits(b) << 16), tag);
  }
}

__device__ __forceinline__ bool tag_ok(u32x2 g, unsigned tag) { return (((g[0] ^ tag) | (g[1] ^ (tag >> 8))) & 0xffu) == 0; }
__device__ __forceinline__ float pk_val(unsigned v) { return __uint_as_float(v & 0xffffff00u); }
template <int I0, int I1>
__device__ __forceinline__ void gather_add(rsrc_t ws, int src, unsigned tag, float* red, const float* xp, float (&x)[7], int w, int lane,
                                           bool pre, u32x4 rq, int c) {
  float s[7];
  for (int spin = 0; spin < SPIN_CAP; spin++) {
    u32x2 g[16];
    MFENCE();
#pragma unroll
    for (int k = 0; k < 16; k++) g[k] = gload(ws, lane * 8 + 512 * (k & 7), src + w * 8192 + (k >> 3) * 4096);
    bool ok = true;
#pragma unroll
    for (int k = 0; k < 4; k++) {
      ok &= tag_ok(g[k], tag) & tag_ok(g[k + 4], tag) & tag_ok(g[k + 8], tag) & tag_ok(g[k + 12], tag);
      s[k] = (pk_val(g[k][0]) + pk_val(g[k + 4][0])) + (pk_val(g[k + 8][0]) + pk_val(g[k + 12][0]));
      if (k < 3) s[k + 4] = (pk_val(g[k][1]) + pk_val(g[k + 4][1])) + (pk_val(g[k + 8][1]) + pk_val(g[k + 12][1]));
    }
    if (__all(ok)) break;
  }
  if (pre) load_wq<I0, I1, true>(rq, c, w, lane);
#pragma unroll
  for (int i = 0; i < 7; i++) { red[w * D + lane + 64 * i] = s[i]; x[i] = xp[64 * i]; }
  __syncthreads();
#pragma unroll
  for (int i = 0; i < 7; i++) {
    const int n = lane + 64 * i;
    x[i] = bfr(x[i] + bfr((red[n] + red[D + n]) + (red[2 * D + n] + red[3 * D + n])));
  }
}
__device__ __forceinline__ void reduce_publish(const f32x4 (&acc)[7], float* red, rsrc_t ws, int dst, unsigned tag, int tid, int w, int lane) {
  put448(red + w * D, acc, lane);
  __syncthreads();
  {
    const int n = tid < D - 256 ? tid + 256 : tid;
    const unsigned a = __float_as_uint((red[tid] + red[D + tid]) + (red[2 * D + tid] + red[3 * D + tid]));
    const unsigned b = __float_as_uint((red[n] + red[D + n]) + (red[2 * D + n] + red[3 * D + n]));
    gstore(ws, tid * 8, dst, ((a + 0x80u) & 0xffffff00u) | (tag & 0xffu), ((b + 0x80u) & 0xffffff00u) | ((tag >> 8) & 0xffu));
  }
}

__global__ void __launch_bounds__(256, 1)
fused_decode(const unsigned short* __restrict__ xin, const char* __restrict__ kc, const char* __restrict__ vc,
             const unsigned short* __restrict__ wq, const unsigned short* __restrict__ wo, const unsigned short* __restrict__ wg,
             const unsigned short* __restrict__ wu, const unsigned short* __restrict__ wd, unsigned short* __restrict__ out, char* wsp) {
  __shared__ __attribute__((aligned(16))) char lds[163840];
  int tid = threadIdx.x, lane = tid & 63;
  const int w = __builtin_amdgcn_readfirstlane(tid >> 6);
  const int bid = blockIdx.x;
  if (bid < NATT) {
    const int head = bid / 15, j = bid % 15;
    const char* kb = kc + (size_t)head * 8388608;
    const char* vb = vc + (size_t)head * 8388608;
    const char* qp = wsp + WS_Q + head * 1024;
    const char* pp = wsp + WS_P + (head * 15 + j) * 2080;
    const int blk0 = (j * 4 + w) * NBLK;
    const unsigned lbase = (unsigned)(size_t)(__attribute__((address_space(3))) char*)lds;
    asm volatile(
  "s_nop 4\n"
  "s_load_dword s91, %7, 0x0\n"
  "s_mov_b64 s[36:37], %0\n"
  "s_mov_b32 s38, 0x800000\n"
  "s_mov_b32 s39, 0x20000\n"
  "s_mov_b64 s[40:41], %1\n"
  "s_mov_b32 s42, 0x800000\n"
  "s_mov_b32 s43, 0x20000\n"
  "s_add_u32 s44, s36, 0x8000000\n"
  "s_addc_u32 s45, s37, 0\n"
  "s_mov_b32 s46, 0x800000\n"
  "s_mov_b32 s47, 0x20000\n"
  "s_add_u32 s48, s40, 0x8000000\n"
  "s_addc_u32 s49, s41, 0\n"
  "s_mov_b32 s50, 0x800000\n"
  "s_mov_b32 s51, 0x20000\n"
  "s_mov_b64 s[68:69], %2\n"
  "s_mov_b64 s[70:71], %3\n"
  "s_lshl_b32 s60, %4, 10\n"
  "s_add_u32 s61, s60, 74752\n"
  "s_sub_i32 s62, 8119, %4\n"
  "s_min_i32 s62, s62, 64\n"
  "s_mov_b32 s76, %5\n"
  "s_mul_i32 s75, s76, 38912\n"
  "s_add_u32 s75, s75, %6\n"
  "s_add_u32 s90, %6, 158208\n"
  "s_mul_i32 s80, s76, 640\n"
  "s_add_u32 s80, s80, %6\n"
  "s_add_u32 s80, s80, 155648\n"
  "s_mul_i32 s79, s76, 400\n"
  "s_add_u32 s79, s79, 2560\n"
  "s_mov_b32 s78, 0x3db8aa3b\n"
  "v_mbcnt_lo_u32_b32 v22, -1, 0\n"
  "v_mbcnt_hi_u32_b32 v22, -1, v22\n"
  "v_lshlrev_b32 v0, 4, v22\n"
  "s_add_u32 s66, s68, 0\n"
  "s_addc_u32 s67, s69, 0\n"
  "v_mbcnt_lo_u32_b32 v30, -1, 0\n"
  "v_mbcnt_hi_u32_b32 v30, -1, v30\n"
  "s_mov_b64 s[52:53], s[36:37]\n"
  "s_mov_b64 s[54:55], s[38:39]\n"
  "s_mov_b32 s56, s61\n"
  "s_call_b64 s[58:59], L_LK0\n"
  "v_add_u32 v1, s75, v0\n"
  "v_lshlrev_b32 v4, 2, v30\n"
  "v_add_u32 v4, s80, v4\n"
  "v_lshrrev_b32 v31, 5, v30\n"
  "v_mul_u32_u24 v31, 320, v31\n"
  "v_add_u32 v3, s80, v31\n"
  "v_bfe_u32 v31, v30, 4, 1\n"
  "v_bfe_u32 v32, v30, 3, 1\n"
  "v_bfe_u32 v33, v30, 2, 1\n"
  "v_lshl_add_u32 v2, v32, 1, v31\n"
  "v_lshl_add_u32 v2, v33, 2, v2\n"
  "v_lshl_add_u32 v2, v2, 2, v3\n"
  "v_and_b32 v33, 1, v30\n"
  "v_lshlrev_b32 v33, 4, v33\n"
  "v_mov_b32 v9, 0xffff\n"
  "v_lshlrev_b32 v33, v33, v9\n"
  "v_cmp_eq_u32 vcc, v31, v32\n"
  "s_nop 1\n"
  "v_cndmask_b32 v33, 0, v33, vcc\n"
  "v_bfe_u32 v31, v30, 1, 2\n"
  "v_cmp_eq_u32 vcc, 0, v31\n"
  "s_nop 1\n"
  "v_cndmask_b32 v5, 0, v33, vcc\n"
  "v_cmp_eq_u32 vcc, 1, v31\n"
  "s_nop 1\n"
  "v_cndmask_b32 v6, 0, v33, vcc\n"
  "v_cmp_eq_u32 vcc, 2, v31\n"
  "s_nop 1\n"
  "v_cndmask_b32 v7, 0, v33, vcc\n"
  "v_cmp_eq_u32 vcc, 3, v31\n"
  "s_nop 1\n"
  "v_cndmask_b32 v8, 0, v33, vcc\n"
  "v_mov_b32 v10, 0\n"
  "v_mov_b32 v11, 0\n"
  "v_mov_b32 v12, 0\n"
  "v_mov_b32 v13, 0\n"
  "v_mov_b32 v9, 0xff800000\n"
  "v_mov_b32 v46, 0\n"
  "s_memrealtime s[2:3]\n"
  "s_waitcnt lgkmcnt(0)\n"
  "s_sub_u32 s34, s2, 224\n"
  "s_call_b64 s[58:59], L_PACE\n"
  "s_call_b64 s[58:59], L_LK1\n"
  "s_waitcnt vmcnt(16)\n"
  "s_call_b64 s[58:59], L_PACE\n"
  "s_call_b64 s[58:59], L_LK2\n"
  "s_waitcnt vmcnt(16)\n"
  "s_call_b64 s[58:59], L_PACE\n"
  "s_call_b64 s[58:59], L_LK3\n"
  "s_waitcnt vmcnt(16)\n"
  "s_call_b64 s[58:59], L_PACE\n"
  "s_call_b64 s[58:59], L_LK4\n"
  "s_waitcnt vmcnt(16)\n"
  "s_call_b64 s[58:59], L_PACE\n"
  "s_call_b64 s[58:59], L_LK5\n"
  "s_waitcnt vmcnt(16)\n"
  "s_call_b64 s[58:59], L_PACE\n"
  "s_call_b64 s[58:59], L_LK6\n"
  "s_waitcnt vmcnt(16)\n"
  "s_call_b64 s[58:59], L_PACE\n"
  "s_call_b64 s[58:59], L_LK7\n"
  "s_waitcnt vmcnt(16)\n"
  "s_mov_b64 s[52:53], s[40:41]\n"
  "s_mov_b64 s[54:55], s[42:43]\n"
  "s_mov_b32 s56, s61\n"
  "s_call_b64 s[58:59], L_PACE\n"
  "s_call_b64 s[58:59], L_LV0\n"
  "s_waitcnt vmcnt(16)\n"
  "s_call_b64 s[58:59], L_PACE\n"
  "s_call_b64 s[58:59], L_LV1\n"
  "s_waitcnt vmcnt(16)\n"
  "s_call_b64 s[58:59], L_PACE\n"
  "s_call_b64 s[58:59], L_LV2\n"
  "s_waitcnt vmcnt(16)\n"
  "s_call_b64 s[58:59], L_PACE\n"
  "s_call_b64 s[58:59], L_LV3\n"
  "s_waitcnt vmcnt(16)\n"
  "s_call_b64 s[58:59], L_PACE\n"
  "s_call_b64 s[58:59], L_LV4\n"
  "s_waitcnt vmcnt(16)\n"
  "s_call_b64 s[58:59], L_PACE\n"
  "s_call_b64 s[58:59], L_LV5\n"
  "s_waitcnt vmcnt(16)\n"
  "s_call_b64 s[58:59], L_QISS\n"
  "s_call_b64 s[58:59], L_PACE\n"
  "s_call_b64 s[58:59], L_LV6\n"
  "s_waitcnt vmcnt(16)\n"
  "s_call_b64 s[58:59], L_PACE\n"
  "s_call_b64 s[58:59], L_LV7\n"
  "s_waitcnt vmcnt(16)\n"
  "s_waitcnt lgkmcnt(0)\n"
  "s_call_b64 s[58:59], L_QCHK\n"
  "s_mov_b64 s[52:53], s[36:37]\n"
  "s_mov_b64 s[54:55], s[38:39]\n"
  "s_mov_b32 s56, s60\n"
  "s_call_b64 s[58:59], L_KC0\n"
  "s_call_b64 s[58:59], L_PACE\n"
  "s_call_b64 s[58:59], L_LK0\n"
  "s_waitcnt vmcnt(16)\n"
  "s_call_b64 s[58:59], L_KC1\n"
  "s_call_b64 s[58:59], L_PACE\n"
  "s_call_b64 s[58:59], L_LK1\n"
  "s_waitcnt vmcnt(16)\n"
  "s_call_b64 s[58:59], L_KC2\n"
  "s_call_b64 s[58:59], L_PACE\n"
  "s_call_b64 s[58:59], L_LK2\n"
  "s_waitcnt vmcnt(16)\n"
  "s_call_b64 s[58:59], L_KC3\n"
  "s_call_b64 s[58:59], L_PACE\n"
  "s_call_b64 s[58:59], L_LK3\n"
  "s_waitcnt vmcnt(16)\n"
  "s_call_b64 s[58:59], L_KC4\n"
  "s_call_b64 s[58:59], L_PACE\n"
  "s_call_b64 s[58:59], L_LK4\n"
  "s_waitcnt vmcnt(16)\n"
  "s_call_b64 s[58:59], L_KC5\n"
  "s_call_b64 s[58:59], L_PACE\n"
  "s_call_b64 s[58:59], L_LK5\n"
  "s_waitcnt vmcnt(16)\n"
  "s_call_b64 s[58:59], L_KC6\n"
  "s_call_b64 s[58:59], L_PACE\n"
  "s_call_b64 s[58:59], L_LK6\n"
  "s_waitcnt vmcnt(16)\n"
  "s_call_b64 s[58:59], L_KC7\n"
  "s_call_b64 s[58:59], L_PACE\n"
  "s_call_b64 s[58:59], L_LK7\n"
  "s_waitcnt vmcnt(16)\n"
  "s_call_b64 s[58:59], L_PACE\n"
  "s_call_b64 s[58:59], L_LK8\n"
  "s_waitcnt vmcnt(16)\n"
  "s_mov_b32 s63, s62\n"
  "s_call_b64 s[58:59], L_SMX\n"
  "s_mov_b64 s[52:53], s[40:41]\n"
  "s_mov_b64 s[54:55], s[42:43]\n"
  "s_mov_b32 s56, s60\n"
  "v_add_u32 v2, s79, v2\n"
  "s_call_b64 s[58:59], L_VC0\n"
  "s_call_b64 s[58:59], L_PACE\n"
  "s_call_b64 s[58:59], L_LV0\n"
  "s_waitcnt vmcnt(16)\n"
  "s_call_b64 s[58:59], L_VC1\n"
  "s_call_b64 s[58:59], L_PACE\n"
  "s_call_b64 s[58:59], L_LV1\n"
  "s_waitcnt vmcnt(16)\n"
  "s_call_b64 s[58:59], L_VC2\n"
  "s_call_b64 s[58:59], L_PACE\n"
  "s_call_b64 s[58:59], L_LV2\n"
  "s_call_b64 s[58:59], L_KC0\n"
  "s_call_b64 s[58:59], L_KC1\n"
  "s_waitcnt vmcnt(16)\n"
  "s_call_b64 s[58:59], L_VC3\n"
  "s_call_b64 s[58:59], L_PACE\n"
  "s_call_b64 s[58:59], L_LV3\n"
  "s_call_b64 s[58:59], L_KC2\n"
  "s_waitcnt vmcnt(16)\n"
  "s_call_b64 s[58:59], L_VC4\n"
  "s_call_b64 s[58:59], L_PACE\n"
  "s_call_b64 s[58:59], L_LV4\n"
  "s_call_b64 s[58:59], L_KC3\n"
  "s_waitcnt vmcnt(16)\n"
  "s_call_b64 s[58:59], L_VC5\n"
  "s_call_b64 s[58:59], L_PACE\n"
  "s_call_b64 s[58:59], L_LV5\n"
  "s_call_b64 s[58:59], L_KC4\n"
  "s_waitcnt vmcnt(16)\n"
  "s_call_b64 s[58:59], L_VC6\n"
  "s_call_b64 s[58:59], L_PACE\n"
  "s_call_b64 s[58:59], L_LV6\n"
  "s_call_b64 s[58:59], L_KC5\n"
  "s_waitcnt vmcnt(16)\n"
  "s_call_b64 s[58:59], L_VC7\n"
  "s_call_b64 s[58:59], L_PACE\n"
  "s_call_b64 s[58:59], L_LV7\n"
  "s_call_b64 s[58:59], L_KC6\n"
  "s_waitcnt vmcnt(16)\n"
  "s_call_b64 s[58:59], L_PACE\n"
  "s_call_b64 s[58:59], L_LV8\n"
  "s_call_b64 s[58:59], L_KC7\n"
  "s_waitcnt vmcnt(16)\n"
  "s_mov_b64 s[52:53], s[44:45]\n"
  "s_mov_b64 s[54:55], s[46:47]\n"
  "s_mov_b32 s56, s61\n"
  "s_add_u32 s57, s56, 4096\n"
  "buffer_load_dwordx4 v[48:51], v0, s[52:55], s56 offen offset:0 nt\n"
  "buffer_load_dwordx4 v[52:55], v0, s[52:55], s56 offen offset:1024 nt\n"
  "buffer_load_dwordx4 v[56:59], v0, s[52:55], s56 offen offset:2048 nt\n"
  "buffer_load_dwordx4 v[60:63], v0, s[52:55], s56 offen offset:3072 nt\n"
  "buffer_load_dwordx4 v[64:67], v0, s[52:55], s57 offen offset:0 nt\n"
  "buffer_load_dwordx4 v[68:71], v0, s[52:55], s57 offen offset:1024 nt\n"
  "buffer_load_dwordx4 v[72:75], v0, s[52:55], s57 offen offset:2048 nt\n"
  "buffer_load_dwordx4 v[76:79], v0, s[52:55], s57 offen offset:3072 nt\n"
  "s_add_u32 s56, s56, 8192\n"
  "v_add_u32 v3, s79, v3\n"
  "v_add_u32 v4, s79, v4\n"
  "s_add_u32 s80, s80, s79\n"
  "s_call_b64 s[58:59], L_KC8\n"
  "s_mov_b32 s63, 73\n"
  "s_call_b64 s[58:59], L_SMX\n"
  "s_add_u32 s66, s68, 16384\n"
  "s_addc_u32 s67, s69, 0\n"
  "s_call_b64 s[58:59], L_QISS\n"
  "s_call_b64 s[58:59], L_VC0\n"
  "s_call_b64 s[58:59], L_VC1\n"
  "s_call_b64 s[58:59], L_VC2\n"
  "s_call_b64 s[58:59], L_VC3\n"
  "s_call_b64 s[58:59], L_VC4\n"
  "s_call_b64 s[58:59], L_VC5\n"
  "s_call_b64 s[58:59], L_VC6\n"
  "s_waitcnt vmcnt(19)\n"
  "s_call_b64 s[58:59], L_VC7\n"
  "s_waitcnt vmcnt(10)\n"
  "s_call_b64 s[58:59], L_VC8\n"
  "v_subrev_u32 v2, s79, v2\n"
  "v_subrev_u32 v3, s79, v3\n"
  "v_subrev_u32 v4, s79, v4\n"
  "s_sub_u32 s80, s80, s79\n"
  "s_waitcnt vmcnt(0)\n"
  "s_call_b64 s[58:59], L_QCHK\n"
  "s_add_u32 s72, s70, 0\n"
  "s_addc_u32 s73, s71, 0\n"
  "s_add_u32 s74, s91, 1\n"
  "s_call_b64 s[58:59], L_PUB\n"
  "v_mov_b32 v10, 0\n"
  "v_mov_b32 v11, 0\n"
  "v_mov_b32 v12, 0\n"
  "v_mov_b32 v13, 0\n"
  "v_mov_b32 v9, 0xff800000\n"
  "v_mov_b32 v46, 0\n"
  "s_add_u32 s34, s34, 388\n"
  "s_call_b64 s[58:59], L_PACE\n"
  "s_mov_b64 s[86:87], s[48:49]\n"
  "s_add_u32 s66, s68, 32768\n"
  "s_addc_u32 s67, s69, 0\n"
  "s_call_b64 s[58:59], L_STREAM\n"
  "s_nop 7\n"
  "v_swap_b32 v10, v14\n"
  "v_swap_b32 v11, v15\n"
  "v_swap_b32 v12, v16\n"
  "v_swap_b32 v13, v17\n"
  "v_swap_b32 v46, v47\n"
  "v_readfirstlane_b32 s57, v9\n"
  "v_mov_b32 v9, s64\n"
  "s_nop 1\n"
  "s_mov_b32 s64, s57\n"
  "v_mov_b32 v10, 0\n"
  "v_mov_b32 v11, 0\n"
  "v_mov_b32 v12, 0\n"
  "v_mov_b32 v13, 0\n"
  "v_mov_b32 v9, 0xff800000\n"
  "v_mov_b32 v46, 0\n"
  "s_call_b64 s[58:59], L_QCHK\n"
  "s_mov_b64 s[52:53], s[44:45]\n"
  "s_mov_b64 s[54:55], s[46:47]\n"
  "s_mov_b32 s56, s60\n"
  "s_call_b64 s[58:59], L_KC0\n"
  "s_call_b64 s[58:59], L_LK0\n"
  "s_waitcnt vmcnt(16)\n"
  "s_call_b64 s[58:59], L_KC1\n"
  "s_call_b64 s[58:59], L_LK1\n"
  "s_waitcnt vmcnt(16)\n"
  "s_call_b64 s[58:59], L_KC2\n"
  "s_call_b64 s[58:59], L_LK2\n"
  "s_waitcnt vmcnt(16)\n"
  "s_call_b64 s[58:59], L_KC3\n"
  "s_call_b64 s[58:59], L_LK3\n"
  "s_waitcnt vmcnt(16)\n"
  "s_call_b64 s[58:59], L_KC4\n"
  "s_call_b64 s[58:59], L_LK4\n"
  "s_waitcnt vmcnt(16)\n"
  "s_call_b64 s[58:59], L_KC5\n"
  "s_call_b64 s[58:59], L_LK5\n"
  "s_waitcnt vmcnt(16)\n"
  "s_call_b64 s[58:59], L_KC6\n"
  "s_call_b64 s[58:59], L_LK6\n"
  "s_waitcnt vmcnt(16)\n"
  "s_call_b64 s[58:59], L_KC7\n"
  "s_call_b64 s[58:59], L_LK7\n"
  "s_waitcnt vmcnt(16)\n"
  "s_call_b64 s[58:59], L_KC8\n"
  "s_call_b64 s[58:59], L_LK8\n"
  "s_waitcnt vmcnt(16)\n"
  "s_mov_b32 s63, 73\n"
  "s_call_b64 s[58:59], L_SMX\n"
  "s_add_u32 s66, s68, 16384\n"
  "s_addc_u32 s67, s69, 0\n"
  "s_call_b64 s[58:59], L_QISS\n"
  "s_mov_b64 s[52:53], s[48:49]\n"
  "s_mov_b64 s[54:55], s[50:51]\n"
  "s_mov_b32 s56, s60\n"
  "v_add_u32 v2, s79, v2\n"
  "s_call_b64 s[58:59], L_VC0\n"
  "s_call_b64 s[58:59], L_LV0\n"
  "s_waitcnt vmcnt(16)\n"
  "s_call_b64 s[58:59], L_VC1\n"
  "s_call_b64 s[58:59], L_LV1\n"
  "s_waitcnt vmcnt(16)\n"
  "s_call_b64 s[58:59], L_QCHK\n"
  "s_call_b64 s[58:59], L_VC2\n"
  "s_call_b64 s[58:59], L_LV2\n"
  "s_call_b64 s[58:59], L_KC0\n"
  "s_call_b64 s[58:59], L_KC1\n"
  "s_waitcnt vmcnt(16)\n"
  "s_call_b64 s[58:59], L_VC3\n"
  "s_call_b64 s[58:59], L_LV3\n"
  "s_call_b64 s[58:59], L_KC2\n"
  "s_waitcnt vmcnt(16)\n"
  "s_call_b64 s[58:59], L_VC4\n"
  "s_call_b64 s[58:59], L_LV4\n"
  "s_call_b64 s[58:59], L_KC3\n"
  "s_waitcnt vmcnt(16)\n"
  "s_call_b64 s[58:59], L_VC5\n"
  "s_call_b64 s[58:59], L_LV5\n"
  "s_call_b64 s[58:59], L_KC4\n"
  "s_waitcnt vmcnt(16)\n"
  "s_call_b64 s[58:59], L_VC6\n"
  "s_call_b64 s[58:59], L_LV6\n"
  "s_call_b64 s[58:59], L_KC5\n"
  "s_waitcnt vmcnt(16)\n"
  "s_call_b64 s[58:59], L_VC7\n"
  "s_call_b64 s[58:59], L_LV7\n"
  "s_call_b64 s[58:59], L_KC6\n"
  "s_waitcnt vmcnt(16)\n"
  "s_call_b64 s[58:59], L_VC8\n"
  "s_call_b64 s[58:59], L_LV8\n"
  "s_call_b64 s[58:59], L_KC7\n"
  "s_waitcnt vmcnt(16)\n"
  "s_nop 7\n"
  "v_swap_b32 v10, v14\n"
  "v_swap_b32 v11, v15\n"
  "v_swap_b32 v12, v16\n"
  "v_swap_b32 v13, v17\n"
  "v_swap_b32 v46, v47\n"
  "v_readfirstlane_b32 s57, v9\n"
  "v_mov_b32 v9, s64\n"
  "s_nop 1\n"
  "s_mov_b32 s64, s57\n"
  "s_mov_b64 s[52:53], s[36:37]\n"
  "s_mov_b64 s[54:55], s[38:39]\n"
  "s_mov_b32 s56, s61\n"
  "s_add_u32 s57, s56, 4096\n"
  "buffer_load_dwordx4 v[48:51], v0, s[52:55], s56 offen offset:0 nt\n"
  "buffer_load_dwordx4 v[52:55], v0, s[52:55], s56 offen offset:1024 nt\n"
  "buffer_load_dwordx4 v[56:59], v0, s[52:55], s56 offen offset:2048 nt\n"
  "buffer_load_dwordx4 v[60:63], v0, s[52:55], s56 offen offset:3072 nt\n"
  "buffer_load_dwordx4 v[64:67], v0, s[52:55], s57 offen offset:0 nt\n"
  "buffer_load_dwordx4 v[68:71], v0, s[52:55], s57 offen offset:1024 nt\n"
  "buffer_load_dwordx4 v[72:75], v0, s[52:55], s57 offen offset:2048 nt\n"
  "buffer_load_dwordx4 v[76:79], v0, s[52:55], s57 offen offset:3072 nt\n"
  "s_add_u32 s56, s56, 8192\n"
  "v_add_u32 v3, s79, v3\n"
  "v_add_u32 v4, s79, v4\n"
  "s_add_u32 s80, s80, s79\n"
  "s_call_b64 s[58:59], L_KC8\n"
  "s_mov_b32 s63, 73\n"
  "s_call_b64 s[58:59], L_SMX\n"
  "s_add_u32 s66, s68, 32768\n"
  "s_addc_u32 s67, s69, 0\n"
  "s_call_b64 s[58:59], L_QISS\n"
  "s_call_b64 s[58:59], L_VC0\n"
  "s_call_b64 s[58:59], L_VC1\n"
  "s_call_b64 s[58:59], L_VC2\n"
  "s_call_b64 s[58:59], L_VC3\n"
  "s_call_b64 s[58:59], L_VC4\n"
  "s_call_b64 s[58:59], L_VC5\n"
  "s_call_b64 s[58:59], L_VC6\n"
  "s_waitcnt vmcnt(19)\n"
  "s_call_b64 s[58:59], L_VC7\n"
  "s_waitcnt vmcnt(10)\n"
  "s_call_b64 s[58:59], L_VC8\n"
  "v_subrev_u32 v2, s79, v2\n"
  "v_subrev_u32 v3, s79, v3\n"
  "v_subrev_u32 v4, s79, v4\n"
  "s_sub_u32 s80, s80, s79\n"
  "s_waitcnt vmcnt(0)\n"
  "s_call_b64 s[58:59], L_QCHK\n"
  "s_add_u32 s72, s70, 0\n"
  "s_addc_u32 s73, s71, 0\n"
  "s_add_u32 s74, s91, 2\n"
  "s_call_b64 s[58:59], L_PUB\n"
  "s_nop 7\n"
  "v_swap_b32 v10, v14\n"
  "v_swap_b32 v11, v15\n"
  "v_swap_b32 v12, v16\n"
  "v_swap_b32 v13, v17\n"
  "v_swap_b32 v46, v47\n"
  "v_readfirstlane_b32 s57, v9\n"
  "v_mov_b32 v9, s64\n"
  "s_nop 1\n"
  "s_mov_b32 s64, s57\n"
  "s_mov_b64 s[86:87], s[40:41]\n"
  "s_add_u32 s66, s68, 49152\n"
  "s_addc_u32 s67, s69, 0\n"
  "s_call_b64 s[58:59], L_STREAM\n"
  "s_add_u32 s72, s70, 499200\n"
  "s_addc_u32 s73, s71, 0\n"
  "s_add_u32 s74, s91, 3\n"
  "s_call_b64 s[58:59], L_PUB\n"
  "v_mov_b32 v10, 0\n"
  "v_mov_b32 v11, 0\n"
  "v_mov_b32 v12, 0\n"
  "v_mov_b32 v13, 0\n"
  "v_mov_b32 v9, 0xff800000\n"
  "v_mov_b32 v46, 0\n"
  "s_call_b64 s[58:59], L_QCHK\n"
  "s_mov_b64 s[52:53], s[44:45]\n"
  "s_mov_b64 s[54:55], s[46:47]\n"
  "s_mov_b32 s56, s61\n"
  "s_call_b64 s[58:59], L_KC0\n"
  "s_call_b64 s[58:59], L_LK0\n"
  "s_waitcnt vmcnt(16)\n"
  "s_call_b64 s[58:59], L_KC1\n"
  "s_call_b64 s[58:59], L_LK1\n"
  "s_waitcnt vmcnt(16)\n"
  "s_call_b64 s[58:59], L_KC2\n"
  "s_call_b64 s[58:59], L_LK2\n"
  "s_waitcnt vmcnt(16)\n"
  "s_call_b64 s[58:59], L_KC3\n"
  "s_call_b64 s[58:59], L_LK3\n"
  "s_waitcnt vmcnt(16)\n"
  "s_call_b64 s[58:59], L_KC4\n"
  "s_call_b64 s[58:59], L_LK4\n"
  "s_waitcnt vmcnt(16)\n"
  "s_call_b64 s[58:59], L_KC5\n"
  "s_call_b64 s[58:59], L_LK5\n"
  "s_waitcnt vmcnt(16)\n"
  "s_call_b64 s[58:59], L_KC6\n"
  "s_call_b64 s[58:59], L_LK6\n"
  "s_waitcnt vmcnt(16)\n"
  "s_call_b64 s[58:59], L_KC7\n"
  "s_call_b64 s[58:59], L_LK7\n"
  "s_waitcnt vmcnt(16)\n"
  "s_call_b64 s[58:59], L_KC8\n"
  "s_mov_b32 s63, 73\n"
  "s_call_b64 s[58:59], L_SMX\n"
  "s_mov_b64 s[52:53], s[48:49]\n"
  "s_mov_b64 s[54:55], s[50:51]\n"
  "s_mov_b32 s56, s61\n"
  "v_add_u32 v2, s79, v2\n"
  "s_call_b64 s[58:59], L_VC0\n"
  "s_call_b64 s[58:59], L_LV0\n"
  "s_waitcnt vmcnt(16)\n"
  "s_call_b64 s[58:59], L_VC1\n"
  "s_call_b64 s[58:59], L_LV1\n"
  "s_waitcnt vmcnt(16)\n"
  "s_call_b64 s[58:59], L_VC2\n"
  "s_call_b64 s[58:59], L_LV2\n"
  "s_call_b64 s[58:59], L_KC0\n"
  "s_call_b64 s[58:59], L_KC1\n"
  "s_waitcnt vmcnt(16)\n"
  "s_call_b64 s[58:59], L_VC3\n"
  "s_call_b64 s[58:59], L_LV3\n"
  "s_call_b64 s[58:59], L_KC2\n"
  "s_waitcnt vmcnt(16)\n"
  "s_call_b64 s[58:59], L_VC4\n"
  "s_call_b64 s[58:59], L_LV4\n"
  "s_call_b64 s[58:59], L_KC3\n"
  "s_waitcnt vmcnt(16)\n"
  "s_call_b64 s[58:59], L_VC5\n"
  "s_call_b64 s[58:59], L_LV5\n"
  "s_call_b64 s[58:59], L_KC4\n"
  "s_waitcnt vmcnt(16)\n"
  "s_call_b64 s[58:59], L_VC6\n"
  "s_call_b64 s[58:59], L_LV6\n"
  "s_call_b64 s[58:59], L_KC5\n"
  "s_waitcnt vmcnt(16)\n"
  "s_call_b64 s[58:59], L_VC7\n"
  "s_call_b64 s[58:59], L_LV7\n"
  "s_call_b64 s[58:59], L_KC6\n"
  "s_waitcnt vmcnt(16)\n"
  "s_call_b64 s[58:59], L_VC8\n"
  "s_call_b64 s[58:59], L_KC7\n"
  "v_add_u32 v3, s79, v3\n"
  "v_add_u32 v4, s79, v4\n"
  "s_add_u32 s80, s80, s79\n"
  "s_mov_b32 s63, s62\n"
  "s_call_b64 s[58:59], L_SMX\n"
  "s_call_b64 s[58:59], L_VC0\n"
  "s_call_b64 s[58:59], L_VC1\n"
  "s_call_b64 s[58:59], L_VC2\n"
  "s_call_b64 s[58:59], L_VC3\n"
  "s_call_b64 s[58:59], L_VC4\n"
  "s_call_b64 s[58:59], L_VC5\n"
  "s_waitcnt vmcnt(8)\n"
  "s_call_b64 s[58:59], L_VC6\n"
  "s_waitcnt vmcnt(0)\n"
  "s_call_b64 s[58:59], L_VC7\n"
  "v_subrev_u32 v2, s79, v2\n"
  "v_subrev_u32 v3, s79, v3\n"
  "v_subrev_u32 v4, s79, v4\n"
  "s_sub_u32 s80, s80, s79\n"
  "s_add_u32 s72, s70, 0\n"
  "s_addc_u32 s73, s71, 0\n"
  "s_add_u32 s74, s91, 4\n"
  "s_call_b64 s[58:59], L_PUB\n"
  "s_branch L_END\n"
  "L_LK0:\n"
  "s_add_u32 s57, s56, 0\n"
  "s_add_u32 m0, s75, 0\n"
  "s_nop 0\n"
  "buffer_load_dwordx4 v0, s[52:55], s57 offen offset:0 nt lds\n"
  "buffer_load_dwordx4 v0, s[52:55], s57 offen offset:1024 nt lds\n"
  "buffer_load_dwordx4 v0, s[52:55], s57 offen offset:2048 nt lds\n"
  "buffer_load_dwordx4 v0, s[52:55], s57 offen offset:3072 nt lds\n"
  "s_add_u32 s57, s56, 4096\n"
  "s_add_u32 m0, s75, 4096\n"
  "s_nop 0\n"
  "buffer_load_dwordx4 v0, s[52:55], s57 offen offset:0 nt lds\n"
  "buffer_load_dwordx4 v0, s[52:55], s57 offen offset:1024 nt lds\n"
  "buffer_load_dwordx4 v0, s[52:55], s57 offen offset:2048 nt lds\n"
  "buffer_load_dwordx4 v0, s[52:55], s57 offen offset:3072 nt lds\n"
  "s_setpc_b64 s[58:59]\n"
  "L_LV0:\n"
  "s_add_u32 s57, s56, 0\n"
  "buffer_load_dwordx4 a[0:3], v0, s[52:55], s57 offen offset:0 nt\n"
  "buffer_load_dwordx4 a[4:7], v0, s[52:55], s57 offen offset:1024 nt\n"
  "buffer_load_dwordx4 a[8:11], v0, s[52:55], s57 offen offset:2048 nt\n"
  "buffer_load_dwordx4 a[12:15], v0, s[52:55], s57 offen offset:3072 nt\n"
  "s_add_u32 s57, s56, 4096\n"
  "buffer_load_dwordx4 a[16:19], v0, s[52:55], s57 offen offset:0 nt\n"
  "buffer_load_dwordx4 a[20:23], v0, s[52:55], s57 offen offset:1024 nt\n"
  "buffer_load_dwordx4 a[24:27], v0, s[52:55], s57 offen offset:2048 nt\n"
  "buffer_load_dwordx4 a[28:31], v0, s[52:55], s57 offen offset:3072 nt\n"
  "s_setpc_b64 s[58:59]\n"
  "L_KC0:\n"
  "ds_read_b128 v[30:33], v1 offset:0\n"
  "ds_read_b128 v[34:37], v1 offset:1024\n"
  "ds_read_b128 v[38:41], v1 offset:2048\n"
  "ds_read_b128 v[42:45], v1 offset:3072\n"
  "s_waitcnt lgkmcnt(3)\n"
  "v_dot2_f32_bf16 v22, v30, v18, 0\n"
  "v_dot2_f32_bf16 v22, v31, v19, v22\n"
  "v_dot2_f32_bf16 v22, v32, v20, v22\n"
  "v_dot2_f32_bf16 v22, v33, v21, v22\n"
  "ds_read_b128 v[30:33], v1 offset:4096\n"
  "s_waitcnt lgkmcnt(3)\n"
  "v_dot2_f32_bf16 v23, v34, v18, 0\n"
  "v_dot2_f32_bf16 v23, v35, v19, v23\n"
  "v_dot2_f32_bf16 v23, v36, v20, v23\n"
  "v_dot2_f32_bf16 v23, v37, v21, v23\n"
  "ds_read_b128 v[34:37], v1 offset:5120\n"
  "s_waitcnt lgkmcnt(3)\n"
  "v_dot2_f32_bf16 v24, v38, v18, 0\n"
  "v_dot2_f32_bf16 v24, v39, v19, v24\n"
  "v_dot2_f32_bf16 v24, v40, v20, v24\n"
  "v_dot2_f32_bf16 v24, v41, v21, v24\n"
  "ds_read_b128 v[38:41], v1 offset:6144\n"
  "s_waitcnt lgkmcnt(3)\n"
  "v_dot2_f32_bf16 v25, v42, v18, 0\n"
  "v_dot2_f32_bf16 v25, v43, v19, v25\n"
  "v_dot2_f32_bf16 v25, v44, v20, v25\n"
  "v_dot2_f32_bf16 v25, v45, v21, v25\n"
  "ds_read_b128 v[42:45], v1 offset:7168\n"
  "s_waitcnt lgkmcnt(3)\n"
  "v_dot2_f32_bf16 v26, v30, v18, 0\n"
  "v_dot2_f32_bf16 v26, v31, v19, v26\n"
  "v_dot2_f32_bf16 v26, v32, v20, v26\n"
  "v_dot2_f32_bf16 v26, v33, v21, v26\n"
  "s_waitcnt lgkmcnt(2)\n"
  "v_dot2_f32_bf16 v27, v34, v18, 0\n"
  "v_dot2_f32_bf16 v27, v35, v19, v27\n"
  "v_dot2_f32_bf16 v27, v36, v20, v27\n"
  "v_dot2_f32_bf16 v27, v37, v21, v27\n"
  "s_waitcnt lgkmcnt(1)\n"
  "v_dot2_f32_bf16 v28, v38, v18, 0\n"
  "v_dot2_f32_bf16 v28, v39, v19, v28\n"
  "v_dot2_f32_bf16 v28, v40, v20, v28\n"
  "v_dot2_f32_bf16 v28, v41, v21, v28\n"
  "s_waitcnt lgkmcnt(0)\n"
  "v_dot2_f32_bf16 v29, v42, v18, 0\n"
  "v_dot2_f32_bf16 v29, v43, v19, v29\n"
  "v_dot2_f32_bf16 v29, v44, v20, v29\n"
  "v_dot2_f32_bf16 v29, v45, v21, v29\n"
  "s_nop 2\n"
  "v_permlane16_swap_b32 v22, v23\n"
  "v_permlane16_swap_b32 v24, v25\n"
  "v_permlane16_swap_b32 v26, v27\n"
  "v_permlane16_swap_b32 v28, v29\n"
  "s_nop 1\n"
  "v_add_f32 v22, v22, v23\n"
  "v_add_f32 v24, v24, v25\n"
  "v_add_f32 v26, v26, v27\n"
  "v_add_f32 v28, v28, v29\n"
  "s_nop 1\n"
  "v_add_f32_dpp v22, v22, v22 row_ror:8 row_mask:0xf bank_mask:0x3\n"
  "v_add_f32_dpp v26, v26, v26 row_ror:8 row_mask:0xf bank_mask:0x3\n"
  "v_add_f32_dpp v22, v24, v24 row_ror:8 row_mask:0xf bank_mask:0xc\n"
  "v_add_f32_dpp v26, v28, v28 row_ror:8 row_mask:0xf bank_mask:0xc\n"
  "s_nop 1\n"
  "v_add_f32_dpp v22, v22, v22 row_half_mirror row_mask:0xf bank_mask:0x5\n"
  "v_add_f32_dpp v22, v26, v26 row_half_mirror row_mask:0xf bank_mask:0xa\n"
  "s_nop 1\n"
  "v_add_f32_dpp v22, v22, v22 quad_perm:[1,0,3,2] row_mask:0xf bank_mask:0xf\n"
  "s_nop 1\n"
  "v_add_f32_dpp v22, v22, v22 quad_perm:[2,3,0,1] row_mask:0xf bank_mask:0xf\n"
  "ds_write_b32 v2, v22 offset:0\n"
  "s_setpc_b64 s[58:59]\n"
  "L_VC0:\n"
  "ds_read_b128 v[38:41], v3 offset:0\n"
  "ds_read_b128 v[42:45], v3 offset:16\n"
  "s_waitcnt lgkmcnt(0)\n"
  "v_and_b32 v30, v38, v5\n"
  "v_and_b32 v31, v38, v6\n"
  "v_and_b32 v32, v38, v7\n"
  "v_and_b32 v33, v38, v8\n"
  "v_and_b32 v34, v39, v5\n"
  "v_and_b32 v35, v39, v6\n"
  "v_and_b32 v36, v39, v7\n"
  "v_and_b32 v37, v39, v8\n"
  "v_mfma_f32_16x16x32_bf16 v[10:13], a[0:3], v[30:33], v[10:13]\n"
  "v_and_b32 v30, v40, v5\n"
  "v_and_b32 v31, v40, v6\n"
  "v_and_b32 v32, v40, v7\n"
  "v_and_b32 v33, v40, v8\n"
  "v_mfma_f32_16x16x32_bf16 v[10:13], a[4:7], v[34:37], v[10:13]\n"
  "v_and_b32 v34, v41, v5\n"
  "v_and_b32 v35, v41, v6\n"
  "v_and_b32 v36, v41, v7\n"
  "v_and_b32 v37, v41, v8\n"
  "v_mfma_f32_16x16x32_bf16 v[10:13], a[8:11], v[30:33], v[10:13]\n"
  "v_and_b32 v30, v42, v5\n"
  "v_and_b32 v31, v42, v6\n"
  "v_and_b32 v32, v42, v7\n"
  "v_and_b32 v33, v42, v8\n"
  "v_mfma_f32_16x16x32_bf16 v[10:13], a[12:15], v[34:37], v[10:13]\n"
  "v_and_b32 v34, v43, v5\n"
  "v_and_b32 v35, v43, v6\n"
  "v_and_b32 v36, v43, v7\n"
  "v_and_b32 v37, v43, v8\n"
  "v_mfma_f32_16x16x32_bf16 v[10:13], a[16:19], v[30:33], v[10:13]\n"
  "v_and_b32 v30, v44, v5\n"
  "v_and_b32 v31, v44, v6\n"
  "v_and_b32 v32, v44, v7\n"
  "v_and_b32 v33, v44, v8\n"
  "v_mfma_f32_16x16x32_bf16 v[10:13], a[20:23], v[34:37], v[10:13]\n"
  "v_and_b32 v34, v45, v5\n"
  "v_and_b32 v35, v45, v6\n"
  "v_and_b32 v36, v45, v7\n"
  "v_and_b32 v37, v45, v8\n"
  "v_mfma_f32_16x16x32_bf16 v[10:13], a[24:27], v[30:33], v[10:13]\n"
  "s_nop 1\n"
  "v_mfma_f32_16x16x32_bf16 v[10:13], a[28:31], v[34:37], v[10:13]\n"
  "s_setpc_b64 s[58:59]\n"
  "L_LK1:\n"
  "s_add_u32 s57, s56, 8192\n"
  "s_add_u32 m0, s75, 8192\n"
  "s_nop 0\n"
  "buffer_load_dwordx4 v0, s[52:55], s57 offen offset:0 nt lds\n"
  "buffer_load_dwordx4 v0, s[52:55], s57 offen offset:1024 nt lds\n"
  "buffer_load_dwordx4 v0, s[52:55], s57 offen offset:2048 nt lds\n"
  "buffer_load_dwordx4 v0, s[52:55], s57 offen offset:3072 nt lds\n"
  "s_add_u32 s57, s56, 12288\n"
  "s_add_u32 m0, s75, 12288\n"
  "s_nop 0\n"
  "buffer_load_dwordx4 v0, s[52:55], s57 offen offset:0 nt lds\n"
  "buffer_load_dwordx4 v0, s[52:55], s57 offen offset:1024 nt lds\n"
  "buffer_load_dwordx4 v0, s[52:55], s57 offen offset:2048 nt lds\n"
  "buffer_load_dwordx4 v0, s[52:55], s57 offen offset:3072 nt lds\n"
  "s_setpc_b64 s[58:59]\n"
  "L_LV1:\n"
  "s_add_u32 s57, s56, 8192\n"
  "buffer_load_dwordx4 a[32:35], v0, s[52:55], s57 offen offset:0 nt\n"
  "buffer_load_dwordx4 a[36:39], v0, s[52:55], s57 offen offset:1024 nt\n"
  "buffer_load_dwordx4 a[40:43], v0, s[52:55], s57 offen offset:2048 nt\n"
  "buffer_load_dwordx4 a[44:47], v0, s[52:55], s57 offen offset:3072 nt\n"
  "s_add_u32 s57, s56, 12288\n"
  "buffer_load_dwordx4 a[48:51], v0, s[52:55], s57 offen offset:0 nt\n"
  "buffer_load_dwordx4 a[52:55], v0, s[52:55], s57 offen offset:1024 nt\n"
  "buffer_load_dwordx4 a[56:59], v0, s[52:55], s57 offen offset:2048 nt\n"
  "buffer_load_dwordx4 a[60:63], v0, s[52:55], s57 offen offset:3072 nt\n"
  "s_setpc_b64 s[58:59]\n"
  "L_KC1:\n"
  "ds_read_b128 v[30:33], v1 offset:8192\n"
  "ds_read_b128 v[34:37], v1 offset:9216\n"
  "ds_read_b128 v[38:41], v1 offset:10240\n"
  "ds_read_b128 v[42:45], v1 offset:11264\n"
  "s_waitcnt lgkmcnt(3)\n"
  "v_dot2_f32_bf16 v22, v30, v18, 0\n"
  "v_dot2_f32_bf16 v22, v31, v19, v22\n"
  "v_dot2_f32_bf16 v22, v32, v20, v22\n"
  "v_dot2_f32_bf16 v22, v33, v21, v22\n"
  "ds_read_b128 v[30:33], v1 offset:12288\n"
  "s_waitcnt lgkmcnt(3)\n"
  "v_dot2_f32_bf16 v23, v34, v18, 0\n"
  "v_dot2_f32_bf16 v23, v35, v19, v23\n"
  "v_dot2_f32_bf16 v23, v36, v20, v23\n"
  "v_dot2_f32_bf16 v23, v37, v21, v23\n"
  "ds_read_b128 v[34:37], v1 offset:13312\n"
  "s_waitcnt lgkmcnt(3)\n"
  "v_dot2_f32_bf16 v24, v38, v18, 0\n"
  "v_dot2_f32_bf16 v24, v39, v19, v24\n"
  "v_dot2_f32_bf16 v24, v40, v20, v24\n"
  "v_dot2_f32_bf16 v24, v41, v21, v24\n"
  "ds_read_b128 v[38:41], v1 offset:14336\n"
  "s_waitcnt lgkmcnt(3)\n"
  "v_dot2_f32_bf16 v25, v42, v18, 0\n"
  "v_dot2_f32_bf16 v25, v43, v19, v25\n"
  "v_dot2_f32_bf16 v25, v44, v20, v25\n"
  "v_dot2_f32_bf16 v25, v45, v21, v25\n"
  "ds_read_b128 v[42:45], v1 offset:15360\n"
  "s_waitcnt lgkmcnt(3)\n"
  "v_dot2_f32_bf16 v26, v30, v18, 0\n"
  "v_dot2_f32_bf16 v26, v31, v19, v26\n"
  "v_dot2_f32_bf16 v26, v32, v20, v26\n"
  "v_dot2_f32_bf16 v26, v33, v21, v26\n"
  "s_waitcnt lgkmcnt(2)\n"
  "v_dot2_f32_bf16 v27, v34, v18, 0\n"
  "v_dot2_f32_bf16 v27, v35, v19, v27\n"
  "v_dot2_f32_bf16 v27, v36, v20, v27\n"
  "v_dot2_f32_bf16 v27, v37, v21, v27\n"
  "s_waitcnt lgkmcnt(1)\n"
  "v_dot2_f32_bf16 v28, v38, v18, 0\n"
  "v_dot2_f32_bf16 v28, v39, v19, v28\n"
  "v_dot2_f32_bf16 v28, v40, v20, v28\n"
  "v_dot2_f32_bf16 v28, v41, v21, v28\n"
  "s_waitcnt lgkmcnt(0)\n"
  "v_dot2_f32_bf16 v29, v42, v18, 0\n"
  "v_dot2_f32_bf16 v29, v43, v19, v29\n"
  "v_dot2_f32_bf16 v29, v44, v20, v29\n"
  "v_dot2_f32_bf16 v29, v45, v21, v29\n"
  "s_nop 2\n"
  "v_permlane16_swap_b32 v22, v23\n"
  "v_permlane16_swap_b32 v24, v25\n"
  "v_permlane16_swap_b32 v26, v27\n"
  "v_permlane16_swap_b32 v28, v29\n"
  "s_nop 1\n"
  "v_add_f32 v22, v22, v23\n"
  "v_add_f32 v24, v24, v25\n"
  "v_add_f32 v26, v26, v27\n"
  "v_add_f32 v28, v28, v29\n"
  "s_nop 1\n"
  "v_add_f32_dpp v22, v22, v22 row_ror:8 row_mask:0xf bank_mask:0x3\n"
  "v_add_f32_dpp v26, v26, v26 row_ror:8 row_mask:0xf bank_mask:0x3\n"
  "v_add_f32_dpp v22, v24, v24 row_ror:8 row_mask:0xf bank_mask:0xc\n"
  "v_add_f32_dpp v26, v28, v28 row_ror:8 row_mask:0xf bank_mask:0xc\n"
  "s_nop 1\n"
  "v_add_f32_dpp v22, v22, v22 row_half_mirror row_mask:0xf bank_mask:0x5\n"
  "v_add_f32_dpp v22, v26, v26 row_half_mirror row_mask:0xf bank_mask:0xa\n"
  "s_nop 1\n"
  "v_add_f32_dpp v22, v22, v22 quad_perm:[1,0,3,2] row_mask:0xf bank_mask:0xf\n"
  "s_nop 1\n"
  "v_add_f32_dpp v22, v22, v22 quad_perm:[2,3,0,1] row_mask:0xf bank_mask:0xf\n"
  "ds_write_b32 v2, v22 offset:32\n"
  "s_setpc_b64 s[58:59]\n"
  "L_VC1:\n"
  "ds_read_b128 v[38:41], v3 offset:32\n"
  "ds_read_b128 v[42:45], v3 offset:48\n"
  "s_waitcnt lgkmcnt(0)\n"
  "v_and_b32 v30, v38, v5\n"
  "v_and_b32 v31, v38, v6\n"
  "v_and_b32 v32, v38, v7\n"
  "v_and_b32 v33, v38, v8\n"
  "v_and_b32 v34, v39, v5\n"
  "v_and_b32 v35, v39, v6\n"
  "v_and_b32 v36, v39, v7\n"
  "v_and_b32 v37, v39, v8\n"
  "v_mfma_f32_16x16x32_bf16 v[10:13], a[32:35], v[30:33], v[10:13]\n"
  "v_and_b32 v30, v40, v5\n"
  "v_and_b32 v31, v40, v6\n"
  "v_and_b32 v32, v40, v7\n"
  "v_and_b32 v33, v40, v8\n"
  "v_mfma_f32_16x16x32_bf16 v[10:13], a[36:39], v[34:37], v[10:13]\n"
  "v_and_b32 v34, v41, v5\n"
  "v_and_b32 v35, v41, v6\n"
  "v_and_b32 v36, v41, v7\n"
  "v_and_b32 v37, v41, v8\n"
  "v_mfma_f32_16x16x32_bf16 v[10:13], a[40:43], v[30:33], v[10:13]\n"
  "v_and_b32 v30, v42, v5\n"
  "v_and_b32 v31, v42, v6\n"
  "v_and_b32 v32, v42, v7\n"
  "v_and_b32 v33, v42, v8\n"
  "v_mfma_f32_16x16x32_bf16 v[10:13], a[44:47], v[34:37], v[10:13]\n"
  "v_and_b32 v34, v43, v5\n"
  "v_and_b32 v35, v43, v6\n"
  "v_and_b32 v36, v43, v7\n"
  "v_and_b32 v37, v43, v8\n"
  "v_mfma_f32_16x16x32_bf16 v[10:13], a[48:51], v[30:33], v[10:13]\n"
  "v_and_b32 v30, v44, v5\n"
  "v_and_b32 v31, v44, v6\n"
  "v_and_b32 v32, v44, v7\n"
  "v_and_b32 v33, v44, v8\n"
  "v_mfma_f32_16x16x32_bf16 v[10:13], a[52:55], v[34:37], v[10:13]\n"
  "v_and_b32 v34, v45, v5\n"
  "v_and_b32 v35, v45, v6\n"
  "v_and_b32 v36, v45, v7\n"
  "v_and_b32 v37, v45, v8\n"
  "v_mfma_f32_16x16x32_bf16 v[10:13], a[56:59], v[30:33], v[10:13]\n"
  "s_nop 1\n"
  "v_mfma_f32_16x16x32_bf16 v[10:13], a[60:63], v[34:37], v[10:13]\n"
  "s_setpc_b64 s[58:59]\n"
  "L_LK2:\n"
  "s_add_u32 s57, s56, 16384\n"
  "s_add_u32 m0, s75, 16384\n"
  "s_nop 0\n"
  "buffer_load_dwordx4 v0, s[52:55], s57 offen offset:0 nt lds\n"
  "buffer_load_dwordx4 v0, s[52:55], s57 offen offset:1024 nt lds\n"
  "buffer_load_dwordx4 v0, s[52:55], s57 offen offset:2048 nt lds\n"
  "buffer_load_dwordx4 v0, s[52:55], s57 offen offset:3072 nt lds\n"
  "s_add_u32 s57, s56, 20480\n"
  "s_add_u32 m0, s75, 20480\n"
  "s_nop 0\n"
  "buffer_load_dwordx4 v0, s[52:55], s57 offen offset:0 nt lds\n"
  "buffer_load_dwordx4 v0, s[52:55], s57 offen offset:1024 nt lds\n"
  "buffer_load_dwordx4 v0, s[52:55], s57 offen offset:2048 nt lds\n"
  "buffer_load_dwordx4 v0, s[52:55], s57 offen offset:3072 nt lds\n"
  "s_setpc_b64 s[58:59]\n"
  "L_LV2:\n"
  "s_add_u32 s57, s56, 16384\n"
  "buffer_load_dwordx4 a[64:67], v0, s[52:55], s57 offen offset:0 nt\n"
  "buffer_load_dwordx4 a[68:71], v0, s[52:55], s57 offen offset:1024 nt\n"
  "buffer_load_dwordx4 a[72:75], v0, s[52:55], s57 offen offset:2048 nt\n"
  "buffer_load_dwordx4 a[76:79], v0, s[52:55], s57 offen offset:3072 nt\n"
  "s_add_u32 s57, s56, 20480\n"
  "buffer_load_dwordx4 a[80:83], v0, s[52:55], s57 offen offset:0 nt\n"
  "buffer_load_dwordx4 a[84:87], v0, s[52:55], s57 offen offset:1024 nt\n"
  "buffer_load_dwordx4 a[88:91], v0, s[52:55], s57 offen offset:2048 nt\n"
  "buffer_load_dwordx4 a[92:95], v0, s[52:55], s57 offen offset:3072 nt\n"
  "s_setpc_b64 s[58:59]\n"
  "L_KC2:\n"
  "ds_read_b128 v[30:33], v1 offset:16384\n"
  "ds_read_b128 v[34:37], v1 offset:17408\n"
  "ds_read_b128 v[38:41], v1 offset:18432\n"
  "ds_read_b128 v[42:45], v1 offset:19456\n"
  "s_waitcnt lgkmcnt(3)\n"
  "v_dot2_f32_bf16 v22, v30, v18, 0\n"
  "v_dot2_f32_bf16 v22, v31, v19, v22\n"
  "v_dot2_f32_bf16 v22, v32, v20, v22\n"
  "v_dot2_f32_bf16 v22, v33, v21, v22\n"
  "ds_read_b128 v[30:33], v1 offset:20480\n"
  "s_waitcnt lgkmcnt(3)\n"
  "v_dot2_f32_bf16 v23, v34, v18, 0\n"
  "v_dot2_f32_bf16 v23, v35, v19, v23\n"
  "v_dot2_f32_bf16 v23, v36, v20, v23\n"
  "v_dot2_f32_bf16 v23, v37, v21, v23\n"
  "ds_read_b128 v[34:37], v1 offset:21504\n"
  "s_waitcnt lgkmcnt(3)\n"
  "v_dot2_f32_bf16 v24, v38, v18, 0\n"
  "v_dot2_f32_bf16 v24, v39, v19, v24\n"
  "v_dot2_f32_bf16 v24, v40, v20, v24\n"
  "v_dot2_f32_bf16 v24, v41, v21, v24\n"
  "ds_read_b128 v[38:41], v1 offset:22528\n"
  "s_waitcnt lgkmcnt(3)\n"
  "v_dot2_f32_bf16 v25, v42, v18, 0\n"
  "v_dot2_f32_bf16 v25, v43, v19, v25\n"
  "v_dot2_f32_bf16 v25, v44, v20, v25\n"
  "v_dot2_f32_bf16 v25, v45, v21, v25\n"
  "ds_read_b128 v[42:45], v1 offset:23552\n"
  "s_waitcnt lgkmcnt(3)\n"
  "v_dot2_f32_bf16 v26, v30, v18, 0\n"
  "v_dot2_f32_bf16 v26, v31, v19, v26\n"
  "v_dot2_f32_bf16 v26, v32, v20, v26\n"
  "v_dot2_f32_bf16 v26, v33, v21, v26\n"
  "s_waitcnt lgkmcnt(2)\n"
  "v_dot2_f32_bf16 v27, v34, v18, 0\n"
  "v_dot2_f32_bf16 v27, v35, v19, v27\n"
  "v_dot2_f32_bf16 v27, v36, v20, v27\n"
  "v_dot2_f32_bf16 v27, v37, v21, v27\n"
  "s_waitcnt lgkmcnt(1)\n"
  "v_dot2_f32_bf16 v28, v38, v18, 0\n"
  "v_dot2_f32_bf16 v28, v39, v19, v28\n"
  "v_dot2_f32_bf16 v28, v40, v20, v28\n"
  "v_dot2_f32_bf16 v28, v41, v21, v28\n"
  "s_waitcnt lgkmcnt(0)\n"
  "v_dot2_f32_bf16 v29, v42, v18, 0\n"
  "v_dot2_f32_bf16 v29, v43, v19, v29\n"
  "v_dot2_f32_bf16 v29, v44, v20, v29\n"
  "v_dot2_f32_bf16 v29, v45, v21, v29\n"
  "s_nop 2\n"
  "v_permlane16_swap_b32 v22, v23\n"
  "v_permlane16_swap_b32 v24, v25\n"
  "v_permlane16_swap_b32 v26, v27\n"
  "v_permlane16_swap_b32 v28, v29\n"
  "s_nop 1\n"
  "v_add_f32 v22, v22, v23\n"
  "v_add_f32 v24, v24, v25\n"
  "v_add_f32 v26, v26, v27\n"
  "v_add_f32 v28, v28, v29\n"
  "s_nop 1\n"
  "v_add_f32_dpp v22, v22, v22 row_ror:8 row_mask:0xf bank_mask:0x3\n"
  "v_add_f32_dpp v26, v26, v26 row_ror:8 row_mask:0xf bank_mask:0x3\n"
  "v_add_f32_dpp v22, v24, v24 row_ror:8 row_mask:0xf bank_mask:0xc\n"
  "v_add_f32_dpp v26, v28, v28 row_ror:8 row_mask:0xf bank_mask:0xc\n"
  "s_nop 1\n"
  "v_add_f32_dpp v22, v22, v22 row_half_mirror row_mask:0xf bank_mask:0x5\n"
  "v_add_f32_dpp v22, v26, v26 row_half_mirror row_mask:0xf bank_mask:0xa\n"
  "s_nop 1\n"
  "v_add_f32_dpp v22, v22, v22 quad_perm:[1,0,3,2] row_mask:0xf bank_mask:0xf\n"
  "s_nop 1\n"
  "v_add_f32_dpp v22, v22, v22 quad_perm:[2,3,0,1] row_mask:0xf bank_mask:0xf\n"
  "ds_write_b32 v2, v22 offset:64\n"
  "s_setpc_b64 s[58:59]\n"
  "L_VC2:\n"
  "ds_read_b128 v[38:41], v3 offset:64\n"
  "ds_read_b128 v[42:45], v3 offset:80\n"
  "s_waitcnt lgkmcnt(0)\n"
  "v_and_b32 v30, v38, v5\n"
  "v_and_b32 v31, v38, v6\n"
  "v_and_b32 v32, v38, v7\n"
  "v_and_b32 v33, v38, v8\n"
  "v_and_b32 v34, v39, v5\n"
  "v_and_b32 v35, v39, v6\n"
  "v_and_b32 v36, v39, v7\n"
  "v_and_b32 v37, v39, v8\n"
  "v_mfma_f32_16x16x32_bf16 v[10:13], a[64:67], v[30:33], v[10:13]\n"
  "v_and_b32 v30, v40, v5\n"
  "v_and_b32 v31, v40, v6\n"
  "v_and_b32 v32, v40, v7\n"
  "v_and_b32 v33, v40, v8\n"
  "v_mfma_f32_16x16x32_bf16 v[10:13], a[68:71], v[34:37], v[10:13]\n"
  "v_and_b32 v34, v41, v5\n"
  "v_and_b32 v35, v41, v6\n"
  "v_and_b32 v36, v41, v7\n"
  "v_and_b32 v37, v41, v8\n"
  "v_mfma_f32_16x16x32_bf16 v[10:13], a[72:75], v[30:33], v[10:13]\n"
  "v_and_b32 v30, v42, v5\n"
  "v_and_b32 v31, v42, v6\n"
  "v_and_b32 v32, v42, v7\n"
  "v_and_b32 v33, v42, v8\n"
  "v_mfma_f32_16x16x32_bf16 v[10:13], a[76:79], v[34:37], v[10:13]\n"
  "v_and_b32 v34, v43, v5\n"
  "v_and_b32 v35, v43, v6\n"
  "v_and_b32 v36, v43, v7\n"
  "v_and_b32 v37, v43, v8\n"
  "v_mfma_f32_16x16x32_bf16 v[10:13], a[80:83], v[30:33], v[10:13]\n"
  "v_and_b32 v30, v44, v5\n"
  "v_and_b32 v31, v44, v6\n"
  "v_and_b32 v32, v44, v7\n"
  "v_and_b32 v33, v44, v8\n"
  "v_mfma_f32_16x16x32_bf16 v[10:13], a[84:87], v[34:37], v[10:13]\n"
  "v_and_b32 v34, v45, v5\n"
  "v_and_b32 v35, v45, v6\n"
  "v_and_b32 v36, v45, v7\n"
  "v_and_b32 v37, v45, v8\n"
  "v_mfma_f32_16x16x32_bf16 v[10:13], a[88:91], v[30:33], v[10:13]\n"
  "s_nop 1\n"
  "v_mfma_f32_16x16x32_bf16 v[10:13], a[92:95], v[34:37], v[10:13]\n"
  "s_setpc_b64 s[58:59]\n"
  "L_LK3:\n"
  "s_add_u32 s57, s56, 24576\n"
  "s_add_u32 m0, s75, 24576\n"
  "s_nop 0\n"
  "buffer_load_dwordx4 v0, s[52:55], s57 offen offset:0 nt lds\n"
  "buffer_load_dwordx4 v0, s[52:55], s57 offen offset:1024 nt lds\n"
  "buffer_load_dwordx4 v0, s[52:55], s57 offen offset:2048 nt lds\n"
  "buffer_load_dwordx4 v0, s[52:55], s57 offen offset:3072 nt lds\n"
  "s_add_u32 s57, s56, 28672\n"
  "s_add_u32 m0, s75, 28672\n"
  "s_nop 0\n"
  "buffer_load_dwordx4 v0, s[52:55], s57 offen offset:0 nt lds\n"
  "buffer_load_dwordx4 v0, s[52:55], s57 offen offset:1024 nt lds\n"
  "buffer_load_dwordx4 v0, s[52:55], s57 offen offset:2048 nt lds\n"
  "buffer_load_dwordx4 v0, s[52:55], s57 offen offset:3072 nt lds\n"
  "s_setpc_b64 s[58:59]\n"
  "L_LV3:\n"
  "s_add_u32 s57, s56, 24576\n"
  "buffer_load_dwordx4 a[96:99], v0, s[52:55], s57 offen offset:0 nt\n"
  "buffer_load_dwordx4 a[100:103], v0, s[52:55], s57 offen offset:1024 nt\n"
  "buffer_load_dwordx4 a[104:107], v0, s[52:55], s57 offen offset:2048 nt\n"
  "buffer_load_dwordx4 a[108:111], v0, s[52:55], s57 offen offset:3072 nt\n"
  "s_add_u32 s57, s56, 28672\n"
  "buffer_load_dwordx4 a[112:115], v0, s[52:55], s57 offen offset:0 nt\n"
  "buffer_load_dwordx4 a[116:119], v0, s[52:55], s57 offen offset:1024 nt\n"
  "buffer_load_dwordx4 a[120:123], v0, s[52:55], s57 offen offset:2048 nt\n"
  "buffer_load_dwordx4 a[124:127], v0, s[52:55], s57 offen offset:3072 nt\n"
  "s_setpc_b64 s[58:59]\n"
  "L_KC3:\n"
  "ds_read_b128 v[30:33], v1 offset:24576\n"
  "ds_read_b128 v[34:37], v1 offset:25600\n"
  "ds_read_b128 v[38:41], v1 offset:26624\n"
  "ds_read_b128 v[42:45], v1 offset:27648\n"
  "s_waitcnt lgkmcnt(3)\n"
  "v_dot2_f32_bf16 v22, v30, v18, 0\n"
  "v_dot2_f32_bf16 v22, v31, v19, v22\n"
  "v_dot2_f32_bf16 v22, v32, v20, v22\n"
  "v_dot2_f32_bf16 v22, v33, v21, v22\n"
  "ds_read_b128 v[30:33], v1 offset:28672\n"
  "s_waitcnt lgkmcnt(3)\n"
  "v_dot2_f32_bf16 v23, v34, v18, 0\n"
  "v_dot2_f32_bf16 v23, v35, v19, v23\n"
  "v_dot2_f32_bf16 v23, v36, v20, v23\n"
  "v_dot2_f32_bf16 v23, v37, v21, v23\n"
  "ds_read_b128 v[34:37], v1 offset:29696\n"
  "s_waitcnt lgkmcnt(3)\n"
  "v_dot2_f32_bf16 v24, v38, v18, 0\n"
  "v_dot2_f32_bf16 v24, v39, v19, v24\n"
  "v_dot2_f32_bf16 v24, v40, v20, v24\n"
  "v_dot2_f32_bf16 v24, v41, v21, v24\n"
  "ds_read_b128 v[38:41], v1 offset:30720\n"
  "s_waitcnt lgkmcnt(3)\n"
  "v_dot2_f32_bf16 v25, v42, v18, 0\n"
  "v_dot2_f32_bf16 v25, v43, v19, v25\n"
  "v_dot2_f32_bf16 v25, v44, v20, v25\n"
  "v_dot2_f32_bf16 v25, v45, v21, v25\n"
  "ds_read_b128 v[42:45], v1 offset:31744\n"
  "s_waitcnt lgkmcnt(3)\n"
  "v_dot2_f32_bf16 v26, v30, v18, 0\n"
  "v_dot2_f32_bf16 v26, v31, v19, v26\n"
  "v_dot2_f32_bf16 v26, v32, v20, v26\n"
  "v_dot2_f32_bf16 v26, v33, v21, v26\n"
  "s_waitcnt lgkmcnt(2)\n"
  "v_dot2_f32_bf16 v27, v34, v18, 0\n"
  "v_dot2_f32_bf16 v27, v35, v19, v27\n"
  "v_dot2_f32_bf16 v27, v36, v20, v27\n"
  "v_dot2_f32_bf16 v27, v37, v21, v27\n"
  "s_waitcnt lgkmcnt(1)\n"
  "v_dot2_f32_bf16 v28, v38, v18, 0\n"
  "v_dot2_f32_bf16 v28, v39, v19, v28\n"
  "v_dot2_f32_bf16 v28, v40, v20, v28\n"
  "v_dot2_f32_bf16 v28, v41, v21, v28\n"
  "s_waitcnt lgkmcnt(0)\n"
  "v_dot2_f32_bf16 v29, v42, v18, 0\n"
  "v_dot2_f32_bf16 v29, v43, v19, v29\n"
  "v_dot2_f32_bf16 v29, v44, v20, v29\n"
  "v_dot2_f32_bf16 v29, v45, v21, v29\n"
  "s_nop 2\n"
  "v_permlane16_swap_b32 v22, v23\n"
  "v_permlane16_swap_b32 v24, v25\n"
  "v_permlane16_swap_b32 v26, v27\n"
  "v_permlane16_swap_b32 v28, v29\n"
  "s_nop 1\n"
  "v_add_f32 v22, v22, v23\n"
  "v_add_f32 v24, v24, v25\n"
  "v_add_f32 v26, v26, v27\n"
  "v_add_f32 v28, v28, v29\n"
  "s_nop 1\n"
  "v_add_f32_dpp v22, v22, v22 row_ror:8 row_mask:0xf bank_mask:0x3\n"
  "v_add_f32_dpp v26, v26, v26 row_ror:8 row_mask:0xf bank_mask:0x3\n"
  "v_add_f32_dpp v22, v24, v24 row_ror:8 row_mask:0xf bank_mask:0xc\n"
  "v_add_f32_dpp v26, v28, v28 row_ror:8 row_mask:0xf bank_mask:0xc\n"
  "s_nop 1\n"
  "v_add_f32_dpp v22, v22, v22 row_half_mirror row_mask:0xf bank_mask:0x5\n"
  "v_add_f32_dpp v22, v26, v26 row_half_mirror row_mask:0xf bank_mask:0xa\n"
  "s_nop 1\n"
  "v_add_f32_dpp v22, v22, v22 quad_perm:[1,0,3,2] row_mask:0xf bank_mask:0xf\n"
  "s_nop 1\n"
  "v_add_f32_dpp v22, v22, v22 quad_perm:[2,3,0,1] row_mask:0xf bank_mask:0xf\n"
  "ds_write_b32 v2, v22 offset:96\n"
  "s_setpc_b64 s[58:59]\n"
  "L_VC3:\n"
  "ds_read_b128 v[38:41], v3 offset:96\n"
  "ds_read_b128 v[42:45], v3 offset:112\n"
  "s_waitcnt lgkmcnt(0)\n"
  "v_and_b32 v30, v38, v5\n"
  "v_and_b32 v31, v38, v6\n"
  "v_and_b32 v32, v38, v7\n"
  "v_and_b32 v33, v38, v8\n"
  "v_and_b32 v34, v39, v5\n"
  "v_and_b32 v35, v39, v6\n"
  "v_and_b32 v36, v39, v7\n"
  "v_and_b32 v37, v39, v8\n"
  "v_mfma_f32_16x16x32_bf16 v[10:13], a[96:99], v[30:33], v[10:13]\n"
  "v_and_b32 v30, v40, v5\n"
  "v_and_b32 v31, v40, v6\n"
  "v_and_b32 v32, v40, v7\n"
  "v_and_b32 v33, v40, v8\n"
  "v_mfma_f32_16x16x32_bf16 v[10:13], a[100:103], v[34:37], v[10:13]\n"
  "v_and_b32 v34, v41, v5\n"
  "v_and_b32 v35, v41, v6\n"
  "v_and_b32 v36, v41, v7\n"
  "v_and_b32 v37, v41, v8\n"
  "v_mfma_f32_16x16x32_bf16 v[10:13], a[104:107], v[30:33], v[10:13]\n"
  "v_and_b32 v30, v42, v5\n"
  "v_and_b32 v31, v42, v6\n"
  "v_and_b32 v32, v42, v7\n"
  "v_and_b32 v33, v42, v8\n"
  "v_mfma_f32_16x16x32_bf16 v[10:13], a[108:111], v[34:37], v[10:13]\n"
  "v_and_b32 v34, v43, v5\n"
  "v_and_b32 v35, v43, v6\n"
  "v_and_b32 v36, v43, v7\n"
  "v_and_b32 v37, v43, v8\n"
  "v_mfma_f32_16x16x32_bf16 v[10:13], a[112:115], v[30:33], v[10:13]\n"
  "v_and_b32 v30, v44, v5\n"
  "v_and_b32 v31, v44, v6\n"
  "v_and_b32 v32, v44, v7\n"
  "v_and_b32 v33, v44, v8\n"
  "v_mfma_f32_16x16x32_bf16 v[10:13], a[116:119], v[34:37], v[10:13]\n"
  "v_and_b32 v34, v45, v5\n"
  "v_and_b32 v35, v45, v6\n"
  "v_and_b32 v36, v45, v7\n"
  "v_and_b32 v37, v45, v8\n"
  "v_mfma_f32_16x16x32_bf16 v[10:13], a[120:123], v[30:33], v[10:13]\n"
  "s_nop 1\n"
  "v_mfma_f32_16x16x32_bf16 v[10:13], a[124:127], v[34:37], v[10:13]\n"
  "s_setpc_b64 s[58:59]\n"
  "L_LK4:\n"
  "s_add_u32 s57, s56, 32768\n"
  "s_add_u32 m0, s75, 32768\n"
  "s_nop 0\n"
  "buffer_load_dwordx4 v0, s[52:55], s57 offen offset:0 nt lds\n"
  "buffer_load_dwordx4 v0, s[52:55], s57 offen offset:1024 nt lds\n"
  "buffer_load_dwordx4 v0, s[52:55], s57 offen offset:2048 nt lds\n"
  "buffer_load_dwordx4 v0, s[52:55], s57 offen offset:3072 nt lds\n"
  "s_add_u32 s57, s56, 36864\n"
  "s_add_u32 m0, s75, 36864\n"
  "s_nop 0\n"
  "buffer_load_dwordx4 v0, s[52:55], s57 offen offset:0 nt lds\n"
  "buffer_load_dwordx4 v0, s[52:55], s57 offen offset:1024 nt lds\n"
  "buffer_load_dwordx4 v[80:83], v0, s[52:55], s57 offen offset:2048 nt\n"
  "buffer_load_dwordx4 v[84:87], v0, s[52:55], s57 offen offset:3072 nt\n"
  "s_setpc_b64 s[58:59]\n"
  "L_LV4:\n"
  "s_add_u32 s57, s56, 32768\n"
  "buffer_load_dwordx4 a[128:131], v0, s[52:55], s57 offen offset:0 nt\n"
  "buffer_load_dwordx4 a[132:135], v0, s[52:55], s57 offen offset:1024 nt\n"
  "buffer_load_dwordx4 a[136:139], v0, s[52:55], s57 offen offset:2048 nt\n"
  "buffer_load_dwordx4 a[140:143], v0, s[52:55], s57 offen offset:3072 nt\n"
  "s_add_u32 s57, s56, 36864\n"
  "buffer_load_dwordx4 a[144:147], v0, s[52:55], s57 offen offset:0 nt\n"
  "buffer_load_dwordx4 a[148:151], v0, s[52:55], s57 offen offset:1024 nt\n"
  "buffer_load_dwordx4 a[152:155], v0, s[52:55], s57 offen offset:2048 nt\n"
  "buffer_load_dwordx4 a[156:159], v0, s[52:55], s57 offen offset:3072 nt\n"
  "s_setpc_b64 s[58:59]\n"
  "L_KC4:\n"
  "ds_read_b128 v[30:33], v1 offset:32768\n"
  "ds_read_b128 v[34:37], v1 offset:33792\n"
  "ds_read_b128 v[38:41], v1 offset:34816\n"
  "ds_read_b128 v[42:45], v1 offset:35840\n"
  "s_waitcnt lgkmcnt(3)\n"
  "v_dot2_f32_bf16 v22, v30, v18, 0\n"
  "v_dot2_f32_bf16 v22, v31, v19, v22\n"
  "v_dot2_f32_bf16 v22, v32, v20, v22\n"
  "v_dot2_f32_bf16 v22, v33, v21, v22\n"
  "ds_read_b128 v[30:33], v1 offset:36864\n"
  "s_waitcnt lgkmcnt(3)\n"
  "v_dot2_f32_bf16 v23, v34, v18, 0\n"
  "v_dot2_f32_bf16 v23, v35, v19, v23\n"
  "v_dot2_f32_bf16 v23, v36, v20, v23\n"
  "v_dot2_f32_bf16 v23, v37, v21, v23\n"
  "ds_read_b128 v[34:37], v1 offset:37888\n"
  "s_waitcnt lgkmcnt(3)\n"
  "v_dot2_f32_bf16 v24, v38, v18, 0\n"
  "v_dot2_f32_bf16 v24, v39, v19, v24\n"
  "v_dot2_f32_bf16 v24, v40, v20, v24\n"
  "v_dot2_f32_bf16 v24, v41, v21, v24\n"
  "s_waitcnt lgkmcnt(2)\n"
  "v_dot2_f32_bf16 v25, v42, v18, 0\n"
  "v_dot2_f32_bf16 v25, v43, v19, v25\n"
  "v_dot2_f32_bf16 v25, v44, v20, v25\n"
  "v_dot2_f32_bf16 v25, v45, v21, v25\n"
  "s_waitcnt lgkmcnt(1)\n"
  "v_dot2_f32_bf16 v26, v30, v18, 0\n"
  "v_dot2_f32_bf16 v26, v31, v19, v26\n"
  "v_dot2_f32_bf16 v26, v32, v20, v26\n"
  "v_dot2_f32_bf16 v26, v33, v21, v26\n"
  "s_waitcnt lgkmcnt(0)\n"
  "v_dot2_f32_bf16 v27, v34, v18, 0\n"
  "v_dot2_f32_bf16 v27, v35, v19, v27\n"
  "v_dot2_f32_bf16 v27, v36, v20, v27\n"
  "v_dot2_f32_bf16 v27, v37, v21, v27\n"
  "v_dot2_f32_bf16 v28, v80, v18, 0\n"
  "v_dot2_f32_bf16 v28, v81, v19, v28\n"
  "v_dot2_f32_bf16 v28, v82, v20, v28\n"
  "v_dot2_f32_bf16 v28, v83, v21, v28\n"
  "v_dot2_f32_bf16 v29, v84, v18, 0\n"
  "v_dot2_f32_bf16 v29, v85, v19, v29\n"
  "v_dot2_f32_bf16 v29, v86, v20, v29\n"
  "v_dot2_f32_bf16 v29, v87, v21, v29\n"
  "s_nop 2\n"
  "v_permlane16_swap_b32 v22, v23\n"
  "v_permlane16_swap_b32 v24, v25\n"
  "v_permlane16_swap_b32 v26, v27\n"
  "v_permlane16_swap_b32 v28, v29\n"
  "s_nop 1\n"
  "v_add_f32 v22, v22, v23\n"
  "v_add_f32 v24, v24, v25\n"
  "v_add_f32 v26, v26, v27\n"
  "v_add_f32 v28, v28, v29\n"
  "s_nop 1\n"
  "v_add_f32_dpp v22, v22, v22 row_ror:8 row_mask:0xf bank_mask:0x3\n"
  "v_add_f32_dpp v26, v26, v26 row_ror:8 row_mask:0xf bank_mask:0x3\n"
  "v_add_f32_dpp v22, v24, v24 row_ror:8 row_mask:0xf bank_mask:0xc\n"
  "v_add_f32_dpp v26, v28, v28 row_ror:8 row_mask:0xf bank_mask:0xc\n"
  "s_nop 1\n"
  "v_add_f32_dpp v22, v22, v22 row_half_mirror row_mask:0xf bank_mask:0x5\n"
  "v_add_f32_dpp v22, v26, v26 row_half_mirror row_mask:0xf bank_mask:0xa\n"
  "s_nop 1\n"
  "v_add_f32_dpp v22, v22, v22 quad_perm:[1,0,3,2] row_mask:0xf bank_mask:0xf\n"
  "s_nop 1\n"
  "v_add_f32_dpp v22, v22, v22 quad_perm:[2,3,0,1] row_mask:0xf bank_mask:0xf\n"
  "ds_write_b32 v2, v22 offset:128\n"
  "s_setpc_b64 s[58:59]\n"
  "L_VC4:\n"
  "ds_read_b128 v[38:41], v3 offset:128\n"
  "ds_read_b128 v[42:45], v3 offset:144\n"
  "s_waitcnt lgkmcnt(0)\n"
  "v_and_b32 v30, v38, v5\n"
  "v_and_b32 v31, v38, v6\n"
  "v_and_b32 v32, v38, v7\n"
  "v_and_b32 v33, v38, v8\n"
  "v_and_b32 v34, v39, v5\n"
  "v_and_b32 v35, v39, v6\n"
  "v_and_b32 v36, v39, v7\n"
  "v_and_b32 v37, v39, v8\n"
  "v_mfma_f32_16x16x32_bf16 v[10:13], a[128:131], v[30:33], v[10:13]\n"
  "v_and_b32 v30, v40, v5\n"
  "v_and_b32 v31, v40, v6\n"
  "v_and_b32 v32, v40, v7\n"
  "v_and_b32 v33, v40, v8\n"
  "v_mfma_f32_16x16x32_bf16 v[10:13], a[132:135], v[34:37], v[10:13]\n"
  "v_and_b32 v34, v41, v5\n"
  "v_and_b32 v35, v41, v6\n"
  "v_and_b32 v36, v41, v7\n"
  "v_and_b32 v37, v41, v8\n"
  "v_mfma_f32_16x16x32_bf16 v[10:13], a[136:139], v[30:33], v[10:13]\n"
  "v_and_b32 v30, v42, v5\n"
  "v_and_b32 v31, v42, v6\n"
  "v_and_b32 v32, v42, v7\n"
  "v_and_b32 v33, v42, v8\n"
  "v_mfma_f32_16x16x32_bf16 v[10:13], a[140:143], v[34:37], v[10:13]\n"
  "v_and_b32 v34, v43, v5\n"
  "v_and_b32 v35, v43, v6\n"
  "v_and_b32 v36, v43, v7\n"
  "v_and_b32 v37, v43, v8\n"
  "v_mfma_f32_16x16x32_bf16 v[10:13], a[144:147], v[30:33], v[10:13]\n"
  "v_and_b32 v30, v44, v5\n"
  "v_and_b32 v31, v44, v6\n"
  "v_and_b32 v32, v44, v7\n"
  "v_and_b32 v33, v44, v8\n"
  "v_mfma_f32_16x16x32_bf16 v[10:13], a[148:151], v[34:37], v[10:13]\n"
  "v_and_b32 v34, v45, v5\n"
  "v_and_b32 v35, v45, v6\n"
  "v_and_b32 v36, v45, v7\n"
  "v_and_b32 v37, v45, v8\n"
  "v_mfma_f32_16x16x32_bf16 v[10:13], a[152:155], v[30:33], v[10:13]\n"
  "s_nop 1\n"
  "v_mfma_f32_16x16x32_bf16 v[10:13], a[156:159], v[34:37], v[10:13]\n"
  "s_setpc_b64 s[58:59]\n"
  "L_LK5:\n"
  "s_add_u32 s57, s56, 40960\n"
  "buffer_load_dwordx4 v[88:91], v0, s[52:55], s57 offen offset:0 nt\n"
  "buffer_load_dwordx4 v[92:95], v0, s[52:55], s57 offen offset:1024 nt\n"
  "buffer_load_dwordx4 v[96:99], v0, s[52:55], s57 offen offset:2048 nt\n"
  "buffer_load_dwordx4 v[100:103], v0, s[52:55], s57 offen offset:3072 nt\n"
  "s_add_u32 s57, s56, 45056\n"
  "buffer_load_dwordx4 v[104:107], v0, s[52:55], s57 offen offset:0 nt\n"
  "buffer_load_dwordx4 v[108:111], v0, s[52:55], s57 offen offset:1024 nt\n"
  "buffer_load_dwordx4 v[112:115], v0, s[52:55], s57 offen offset:2048 nt\n"
  "buffer_load_dwordx4 v[116:119], v0, s[52:55], s57 offen offset:3072 nt\n"
  "s_setpc_b64 s[58:59]\n"
  "L_LV5:\n"
  "s_add_u32 s57, s56, 40960\n"
  "buffer_load_dwordx4 a[160:163], v0, s[52:55], s57 offen offset:0 nt\n"
  "buffer_load_dwordx4 a[164:167], v0, s[52:55], s57 offen offset:1024 nt\n"
  "buffer_load_dwordx4 a[168:171], v0, s[52:55], s57 offen offset:2048 nt\n"
  "buffer_load_dwordx4 a[172:175], v0, s[52:55], s57 offen offset:3072 nt\n"
  "s_add_u32 s57, s56, 45056\n"
  "buffer_load_dwordx4 a[176:179], v0, s[52:55], s57 offen offset:0 nt\n"
  "buffer_load_dwordx4 a[180:183], v0, s[52:55], s57 offen offset:1024 nt\n"
  "buffer_load_dwordx4 a[184:187], v0, s[52:55], s57 offen offset:2048 nt\n"
  "buffer_load_dwordx4 a[188:191], v0, s[52:55], s57 offen offset:3072 nt\n"
  "s_setpc_b64 s[58:59]\n"
  "L_KC5:\n"
  "v_dot2_f32_bf16 v22, v88, v18, 0\n"
  "v_dot2_f32_bf16 v22, v89, v19, v22\n"
  "v_dot2_f32_bf16 v22, v90, v20, v22\n"
  "v_dot2_f32_bf16 v22, v91, v21, v22\n"
  "v_dot2_f32_bf16 v23, v92, v18, 0\n"
  "v_dot2_f32_bf16 v23, v93, v19, v23\n"
  "v_dot2_f32_bf16 v23, v94, v20, v23\n"
  "v_dot2_f32_bf16 v23, v95, v21, v23\n"
  "v_dot2_f32_bf16 v24, v96, v18, 0\n"
  "v_dot2_f32_bf16 v24, v97, v19, v24\n"
  "v_dot2_f32_bf16 v24, v98, v20, v24\n"
  "v_dot2_f32_bf16 v24, v99, v21, v24\n"
  "v_dot2_f32_bf16 v25, v100, v18, 0\n"
  "v_dot2_f32_bf16 v25, v101, v19, v25\n"
  "v_dot2_f32_bf16 v25, v102, v20, v25\n"
  "v_dot2_f32_bf16 v25, v103, v21, v25\n"
  "v_dot2_f32_bf16 v26, v104, v18, 0\n"
  "v_dot2_f32_bf16 v26, v105, v19, v26\n"
  "v_dot2_f32_bf16 v26, v106, v20, v26\n"
  "v_dot2_f32_bf16 v26, v107, v21, v26\n"
  "v_dot2_f32_bf16 v27, v108, v18, 0\n"
  "v_dot2_f32_bf16 v27, v109, v19, v27\n"
  "v_dot2_f32_bf16 v27, v110, v20, v27\n"
  "v_dot2_f32_bf16 v27, v111, v21, v27\n"
  "v_dot2_f32_bf16 v28, v112, v18, 0\n"
  "v_dot2_f32_bf16 v28, v113, v19, v28\n"
  "v_dot2_f32_bf16 v28, v114, v20, v28\n"
  "v_dot2_f32_bf16 v28, v115, v21, v28\n"
  "v_dot2_f32_bf16 v29, v116, v18, 0\n"
  "v_dot2_f32_bf16 v29, v117, v19, v29\n"
  "v_dot2_f32_bf16 v29, v118, v20, v29\n"
  "v_dot2_f32_bf16 v29, v119, v21, v29\n"
  "s_nop 2\n"
  "v_permlane16_swap_b32 v22, v23\n"
  "v_permlane16_swap_b32 v24, v25\n"
  "v_permlane16_swap_b32 v26, v27\n"
  "v_permlane16_swap_b32 v28, v29\n"
  "s_nop 1\n"
  "v_add_f32 v22, v22, v23\n"
  "v_add_f32 v24, v24, v25\n"
  "v_add_f32 v26, v26, v27\n"
  "v_add_f32 v28, v28, v29\n"
  "s_nop 1\n"
  "v_add_f32_dpp v22, v22, v22 row_ror:8 row_mask:0xf bank_mask:0x3\n"
  "v_add_f32_dpp v26, v26, v26 row_ror:8 row_mask:0xf bank_mask:0x3\n"
  "v_add_f32_dpp v22, v24, v24 row_ror:8 row_mask:0xf bank_mask:0xc\n"
  "v_add_f32_dpp v26, v28, v28 row_ror:8 row_mask:0xf bank_mask:0xc\n"
  "s_nop 1\n"
  "v_add_f32_dpp v22, v22, v22 row_half_mirror row_mask:0xf bank_mask:0x5\n"
  "v_add_f32_dpp v22, v26, v26 row_half_mirror row_mask:0xf bank_mask:0xa\n"
  "s_nop 1\n"
  "v_add_f32_dpp v22, v22, v22 quad_perm:[1,0,3,2] row_mask:0xf bank_mask:0xf\n"
  "s_nop 1\n"
  "v_add_f32_dpp v22, v22, v22 quad_perm:[2,3,0,1] row_mask:0xf bank_mask:0xf\n"
  "ds_write_b32 v2, v22 offset:160\n"
  "s_setpc_b64 s[58:59]\n"
  "L_VC5:\n"
  "ds_read_b128 v[38:41], v3 offset:160\n"
  "ds_read_b128 v[42:45], v3 offset:176\n"
  "s_waitcnt lgkmcnt(0)\n"
  "v_and_b32 v30, v38, v5\n"
  "v_and_b32 v31, v38, v6\n"
  "v_and_b32 v32, v38, v7\n"
  "v_and_b32 v33, v38, v8\n"
  "v_and_b32 v34, v39, v5\n"
  "v_and_b32 v35, v39, v6\n"
  "v_and_b32 v36, v39, v7\n"
  "v_and_b32 v37, v39, v8\n"
  "v_mfma_f32_16x16x32_bf16 v[10:13], a[160:163], v[30:33], v[10:13]\n"
  "v_and_b32 v30, v40, v5\n"
  "v_and_b32 v31, v40, v6\n"
  "v_and_b32 v32, v40, v7\n"
  "v_and_b32 v33, v40, v8\n"
  "v_mfma_f32_16x16x32_bf16 v[10:13], a[164:167], v[34:37], v[10:13]\n"
  "v_and_b32 v34, v41, v5\n"
  "v_and_b32 v35, v41, v6\n"
  "v_and_b32 v36, v41, v7\n"
  "v_and_b32 v37, v41, v8\n"
  "v_mfma_f32_16x16x32_bf16 v[10:13], a[168:171], v[30:33], v[10:13]\n"
  "v_and_b32 v30, v42, v5\n"
  "v_and_b32 v31, v42, v6\n"
  "v_and_b32 v32, v42, v7\n"
  "v_and_b32 v33, v42, v8\n"
  "v_mfma_f32_16x16x32_bf16 v[10:13], a[172:175], v[34:37], v[10:13]\n"
  "v_and_b32 v34, v43, v5\n"
  "v_and_b32 v35, v43, v6\n"
  "v_and_b32 v36, v43, v7\n"
  "v_and_b32 v37, v43, v8\n"
  "v_mfma_f32_16x16x32_bf16 v[10:13], a[176:179], v[30:33], v[10:13]\n"
  "v_and_b32 v30, v44, v5\n"
  "v_and_b32 v31, v44, v6\n"
  "v_and_b32 v32, v44, v7\n"
  "v_and_b32 v33, v44, v8\n"
  "v_mfma_f32_16x16x32_bf16 v[10:13], a[180:183], v[34:37], v[10:13]\n"
  "v_and_b32 v34, v45, v5\n"
  "v_and_b32 v35, v45, v6\n"
  "v_and_b32 v36, v45, v7\n"
  "v_and_b32 v37, v45, v8\n"
  "v_mfma_f32_16x16x32_bf16 v[10:13], a[184:187], v[30:33], v[10:13]\n"
  "s_nop 1\n"
  "v_mfma_f32_16x16x32_bf16 v[10:13], a[188:191], v[34:37], v[10:13]\n"
  "s_setpc_b64 s[58:59]\n"
  "L_LK6:\n"
  "s_add_u32 s57, s56, 49152\n"
  "buffer_load_dwordx4 v[120:123], v0, s[52:55], s57 offen offset:0 nt\n"
  "buffer_load_dwordx4 v[124:127], v0, s[52:55], s57 offen offset:1024 nt\n"
  "buffer_load_dwordx4 v[128:131], v0, s[52:55], s57 offen offset:2048 nt\n"
  "buffer_load_dwordx4 v[132:135], v0, s[52:55], s57 offen offset:3072 nt\n"
  "s_add_u32 s57, s56, 53248\n"
  "buffer_load_dwordx4 v[136:139], v0, s[52:55], s57 offen offset:0 nt\n"
  "buffer_load_dwordx4 v[140:143], v0, s[52:55], s57 offen offset:1024 nt\n"
  "buffer_load_dwordx4 v[144:147], v0, s[52:55], s57 offen offset:2048 nt\n"
  "buffer_load_dwordx4 v[148:151], v0, s[52:55], s57 offen offset:3072 nt\n"
  "s_setpc_b64 s[58:59]\n"
  "L_LV6:\n"
  "s_add_u32 s57, s56, 49152\n"
  "buffer_load_dwordx4 a[192:195], v0, s[52:55], s57 offen offset:0 nt\n"
  "buffer_load_dwordx4 a[196:199], v0, s[52:55], s57 offen offset:1024 nt\n"
  "buffer_load_dwordx4 a[200:203], v0, s[52:55], s57 offen offset:2048 nt\n"
  "buffer_load_dwordx4 a[204:207], v0, s[52:55], s57 offen offset:3072 nt\n"
  "s_add_u32 s57, s56, 53248\n"
  "buffer_load_dwordx4 a[208:211], v0, s[52:55], s57 offen offset:0 nt\n"
  "buffer_load_dwordx4 a[212:215], v0, s[52:55], s57 offen offset:1024 nt\n"
  "buffer_load_dwordx4 a[216:219], v0, s[52:55], s57 offen offset:2048 nt\n"
  "buffer_load_dwordx4 a[220:223], v0, s[52:55], s57 offen offset:3072 nt\n"
  "s_setpc_b64 s[58:59]\n"
  "L_KC6:\n"
  "v_dot2_f32_bf16 v22, v120, v18, 0\n"
  "v_dot2_f32_bf16 v22, v121, v19, v22\n"
  "v_dot2_f32_bf16 v22, v122, v20, v22\n"
  "v_dot2_f32_bf16 v22, v123, v21, v22\n"
  "v_dot2_f32_bf16 v23, v124, v18, 0\n"
  "v_dot2_f32_bf16 v23, v125, v19, v23\n"
  "v_dot2_f32_bf16 v23, v126, v20, v23\n"
  "v_dot2_f32_bf16 v23, v127, v21, v23\n"
  "v_dot2_f32_bf16 v24, v128, v18, 0\n"
  "v_dot2_f32_bf16 v24, v129, v19, v24\n"
  "v_dot2_f32_bf16 v24, v130, v20, v24\n"
  "v_dot2_f32_bf16 v24, v131, v21, v24\n"
  "v_dot2_f32_bf16 v25, v132, v18, 0\n"
  "v_dot2_f32_bf16 v25, v133, v19, v25\n"
  "v_dot2_f32_bf16 v25, v134, v20, v25\n"
  "v_dot2_f32_bf16 v25, v135, v21, v25\n"
  "v_dot2_f32_bf16 v26, v136, v18, 0\n"
  "v_dot2_f32_bf16 v26, v137, v19, v26\n"
  "v_dot2_f32_bf16 v26, v138, v20, v26\n"
  "v_dot2_f32_bf16 v26, v139, v21, v26\n"
  "v_dot2_f32_bf16 v27, v140, v18, 0\n"
  "v_dot2_f32_bf16 v27, v141, v19, v27\n"
  "v_dot2_f32_bf16 v27, v142, v20, v27\n"
  "v_dot2_f32_bf16 v27, v143, v21, v27\n"
  "v_dot2_f32_bf16 v28, v144, v18, 0\n"
  "v_dot2_f32_bf16 v28, v145, v19, v28\n"
  "v_dot2_f32_bf16 v28, v146, v20, v28\n"
  "v_dot2_f32_bf16 v28, v147, v21, v28\n"
  "v_dot2_f32_bf16 v29, v148, v18, 0\n"
  "v_dot2_f32_bf16 v29, v149, v19, v29\n"
  "v_dot2_f32_bf16 v29, v150, v20, v29\n"
  "v_dot2_f32_bf16 v29, v151, v21, v29\n"
  "s_nop 2\n"
  "v_permlane16_swap_b32 v22, v23\n"
  "v_permlane16_swap_b32 v24, v25\n"
  "v_permlane16_swap_b32 v26, v27\n"
  "v_permlane16_swap_b32 v28, v29\n"
  "s_nop 1\n"
  "v_add_f32 v22, v22, v23\n"
  "v_add_f32 v24, v24, v25\n"
  "v_add_f32 v26, v26, v27\n"
  "v_add_f32 v28, v28, v29\n"
  "s_nop 1\n"
  "v_add_f32_dpp v22, v22, v22 row_ror:8 row_mask:0xf bank_mask:0x3\n"
  "v_add_f32_dpp v26, v26, v26 row_ror:8 row_mask:0xf bank_mask:0x3\n"
  "v_add_f32_dpp v22, v24, v24 row_ror:8 row_mask:0xf bank_mask:0xc\n"
  "v_add_f32_dpp v26, v28, v28 row_ror:8 row_mask:0xf bank_mask:0xc\n"
  "s_nop 1\n"
  "v_add_f32_dpp v22, v22, v22 row_half_mirror row_mask:0xf bank_mask:0x5\n"
  "v_add_f32_dpp v22, v26, v26 row_half_mirror row_mask:0xf bank_mask:0xa\n"
  "s_nop 1\n"
  "v_add_f32_dpp v22, v22, v22 quad_perm:[1,0,3,2] row_mask:0xf bank_mask:0xf\n"
  "s_nop 1\n"
  "v_add_f32_dpp v22, v22, v22 quad_perm:[2,3,0,1] row_mask:0xf bank_mask:0xf\n"
  "ds_write_b32 v2, v22 offset:192\n"
  "s_setpc_b64 s[58:59]\n"
  "L_VC6:\n"
  "ds_read_b128 v[38:41], v3 offset:192\n"
  "ds_read_b128 v[42:45], v3 offset:208\n"
  "s_waitcnt lgkmcnt(0)\n"
  "v_and_b32 v30, v38, v5\n"
  "v_and_b32 v31, v38, v6\n"
  "v_and_b32 v32, v38, v7\n"
  "v_and_b32 v33, v38, v8\n"
  "v_and_b32 v34, v39, v5\n"
  "v_and_b32 v35, v39, v6\n"
  "v_and_b32 v36, v39, v7\n"
  "v_and_b32 v37, v39, v8\n"
  "v_mfma_f32_16x16x32_bf16 v[10:13], a[192:195], v[30:33], v[10:13]\n"
  "v_and_b32 v30, v40, v5\n"
  "v_and_b32 v31, v40, v6\n"
  "v_and_b32 v32, v40, v7\n"
  "v_and_b32 v33, v40, v8\n"
  "v_mfma_f32_16x16x32_bf16 v[10:13], a[196:199], v[34:37], v[10:13]\n"
  "v_and_b32 v34, v41, v5\n"
  "v_and_b32 v35, v41, v6\n"
  "v_and_b32 v36, v41, v7\n"
  "v_and_b32 v37, v41, v8\n"
  "v_mfma_f32_16x16x32_bf16 v[10:13], a[200:203], v[30:33], v[10:13]\n"
  "v_and_b32 v30, v42, v5\n"
  "v_and_b32 v31, v42, v6\n"
  "v_and_b32 v32, v42, v7\n"
  "v_and_b32 v33, v42, v8\n"
  "v_mfma_f32_16x16x32_bf16 v[10:13], a[204:207], v[34:37], v[10:13]\n"
  "v_and_b32 v34, v43, v5\n"
  "v_and_b32 v35, v43, v6\n"
  "v_and_b32 v36, v43, v7\n"
  "v_and_b32 v37, v43, v8\n"
  "v_mfma_f32_16x16x32_bf16 v[10:13], a[208:211], v[30:33], v[10:13]\n"
  "v_and_b32 v30, v44, v5\n"
  "v_and_b32 v31, v44, v6\n"
  "v_and_b32 v32, v44, v7\n"
  "v_and_b32 v33, v44, v8\n"
  "v_mfma_f32_16x16x32_bf16 v[10:13], a[212:215], v[34:37], v[10:13]\n"
  "v_and_b32 v34, v45, v5\n"
  "v_and_b32 v35, v45, v6\n"
  "v_and_b32 v36, v45, v7\n"
  "v_and_b32 v37, v45, v8\n"
  "v_mfma_f32_16x16x32_bf16 v[10:13], a[216:219], v[30:33], v[10:13]\n"
  "s_nop 1\n"
  "v_mfma_f32_16x16x32_bf16 v[10:13], a[220:223], v[34:37], v[10:13]\n"
  "s_setpc_b64 s[58:59]\n"
  "L_LK7:\n"
  "s_add_u32 s57, s56, 57344\n"
  "buffer_load_dwordx4 v[152:155], v0, s[52:55], s57 offen offset:0 nt\n"
  "buffer_load_dwordx4 v[156:159], v0, s[52:55], s57 offen offset:1024 nt\n"
  "buffer_load_dwordx4 v[160:163], v0, s[52:55], s57 offen offset:2048 nt\n"
  "buffer_load_dwordx4 v[164:167], v0, s[52:55], s57 offen offset:3072 nt\n"
  "s_add_u32 s57, s56, 61440\n"
  "buffer_load_dwordx4 v[168:171], v0, s[52:55], s57 offen offset:0 nt\n"
  "buffer_load_dwordx4 v[172:175], v0, s[52:55], s57 offen offset:1024 nt\n"
  "buffer_load_dwordx4 v[176:179], v0, s[52:55], s57 offen offset:2048 nt\n"
  "buffer_load_dwordx4 v[180:183], v0, s[52:55], s57 offen offset:3072 nt\n"
  "s_setpc_b64 s[58:59]\n"
  "L_LV7:\n"
  "s_add_u32 s57, s56, 57344\n"
  "buffer_load_dwordx4 a[224:227], v0, s[52:55], s57 offen offset:0 nt\n"
  "buffer_load_dwordx4 a[228:231], v0, s[52:55], s57 offen offset:1024 nt\n"
  "buffer_load_dwordx4 a[232:235], v0, s[52:55], s57 offen offset:2048 nt\n"
  "buffer_load_dwordx4 a[236:239], v0, s[52:55], s57 offen offset:3072 nt\n"
  "s_add_u32 s57, s56, 61440\n"
  "buffer_load_dwordx4 a[240:243], v0, s[52:55], s57 offen offset:0 nt\n"
  "buffer_load_dwordx4 a[244:247], v0, s[52:55], s57 offen offset:1024 nt\n"
  "buffer_load_dwordx4 a[248:251], v0, s[52:55], s57 offen offset:2048 nt\n"
  "buffer_load_dwordx4 a[252:255], v0, s[52:55], s57 offen offset:3072 nt\n"
  "s_setpc_b64 s[58:59]\n"
  "L_KC7:\n"
  "v_dot2_f32_bf16 v22, v152, v18, 0\n"
  "v_dot2_f32_bf16 v22, v153, v19, v22\n"
  "v_dot2_f32_bf16 v22, v154, v20, v22\n"
  "v_dot2_f32_bf16 v22, v155, v21, v22\n"
  "v_dot2_f32_bf16 v23, v156, v18, 0\n"
  "v_dot2_f32_bf16 v23, v157, v19, v23\n"
  "v_dot2_f32_bf16 v23, v158, v20, v23\n"
  "v_dot2_f32_bf16 v23, v159, v21, v23\n"
  "v_dot2_f32_bf16 v24, v160, v18, 0\n"
  "v_dot2_f32_bf16 v24, v161, v19, v24\n"
  "v_dot2_f32_bf16 v24, v162, v20, v24\n"
  "v_dot2_f32_bf16 v24, v163, v21, v24\n"
  "v_dot2_f32_bf16 v25, v164, v18, 0\n"
  "v_dot2_f32_bf16 v25, v165, v19, v25\n"
  "v_dot2_f32_bf16 v25, v166, v20, v25\n"
  "v_dot2_f32_bf16 v25, v167, v21, v25\n"
  "v_dot2_f32_bf16 v26, v168, v18, 0\n"
  "v_dot2_f32_bf16 v26, v169, v19, v26\n"
  "v_dot2_f32_bf16 v26, v170, v20, v26\n"
  "v_dot2_f32_bf16 v26, v171, v21, v26\n"
  "v_dot2_f32_bf16 v27, v172, v18, 0\n"
  "v_dot2_f32_bf16 v27, v173, v19, v27\n"
  "v_dot2_f32_bf16 v27, v174, v20, v27\n"
  "v_dot2_f32_bf16 v27, v175, v21, v27\n"
  "v_dot2_f32_bf16 v28, v176, v18, 0\n"
  "v_dot2_f32_bf16 v28, v177, v19, v28\n"
  "v_dot2_f32_bf16 v28, v178, v20, v28\n"
  "v_dot2_f32_bf16 v28, v179, v21, v28\n"
  "v_dot2_f32_bf16 v29, v180, v18, 0\n"
  "v_dot2_f32_bf16 v29, v181, v19, v29\n"
  "v_dot2_f32_bf16 v29, v182, v20, v29\n"
  "v_dot2_f32_bf16 v29, v183, v21, v29\n"
  "s_nop 2\n"
  "v_permlane16_swap_b32 v22, v23\n"
  "v_permlane16_swap_b32 v24, v25\n"
  "v_permlane16_swap_b32 v26, v27\n"
  "v_permlane16_swap_b32 v28, v29\n"
  "s_nop 1\n"
  "v_add_f32 v22, v22, v23\n"
  "v_add_f32 v24, v24, v25\n"
  "v_add_f32 v26, v26, v27\n"
  "v_add_f32 v28, v28, v29\n"
  "s_nop 1\n"
  "v_add_f32_dpp v22, v22, v22 row_ror:8 row_mask:0xf bank_mask:0x3\n"
  "v_add_f32_dpp v26, v26, v26 row_ror:8 row_mask:0xf bank_mask:0x3\n"
  "v_add_f32_dpp v22, v24, v24 row_ror:8 row_mask:0xf bank_mask:0xc\n"
  "v_add_f32_dpp v26, v28, v28 row_ror:8 row_mask:0xf bank_mask:0xc\n"
  "s_nop 1\n"
  "v_add_f32_dpp v22, v22, v22 row_half_mirror row_mask:0xf bank_mask:0x5\n"
  "v_add_f32_dpp v22, v26, v26 row_half_mirror row_mask:0xf bank_mask:0xa\n"
  "s_nop 1\n"
  "v_add_f32_dpp v22, v22, v22 quad_perm:[1,0,3,2] row_mask:0xf bank_mask:0xf\n"
  "s_nop 1\n"
  "v_add_f32_dpp v22, v22, v22 quad_perm:[2,3,0,1] row_mask:0xf bank_mask:0xf\n"
  "ds_write_b32 v2, v22 offset:224\n"
  "s_setpc_b64 s[58:59]\n"
  "L_VC7:\n"
  "ds_read_b128 v[38:41], v3 offset:224\n"
  "ds_read_b128 v[42:45], v3 offset:240\n"
  "s_waitcnt lgkmcnt(0)\n"
  "v_and_b32 v30, v38, v5\n"
  "v_and_b32 v31, v38, v6\n"
  "v_and_b32 v32, v38, v7\n"
  "v_and_b32 v33, v38, v8\n"
  "v_and_b32 v34, v39, v5\n"
  "v_and_b32 v35, v39, v6\n"
  "v_and_b32 v36, v39, v7\n"
  "v_and_b32 v37, v39, v8\n"
  "v_mfma_f32_16x16x32_bf16 v[10:13], a[224:227], v[30:33], v[10:13]\n"
  "v_and_b32 v30, v40, v5\n"
  "v_and_b32 v31, v40, v6\n"
  "v_and_b32 v32, v40, v7\n"
  "v_and_b32 v33, v40, v8\n"
  "v_mfma_f32_16x16x32_bf16 v[10:13], a[228:231], v[34:37], v[10:13]\n"
  "v_and_b32 v34, v41, v5\n"
  "v_and_b32 v35, v41, v6\n"
  "v_and_b32 v36, v41, v7\n"
  "v_and_b32 v37, v41, v8\n"
  "v_mfma_f32_16x16x32_bf16 v[10:13], a[232:235], v[30:33], v[10:13]\n"
  "v_and_b32 v30, v42, v5\n"
  "v_and_b32 v31, v42, v6\n"
  "v_and_b32 v32, v42, v7\n"
  "v_and_b32 v33, v42, v8\n"
  "v_mfma_f32_16x16x32_bf16 v[10:13], a[236:239], v[34:37], v[10:13]\n"
  "v_and_b32 v34, v43, v5\n"
  "v_and_b32 v35, v43, v6\n"
  "v_and_b32 v36, v43, v7\n"
  "v_and_b32 v37, v43, v8\n"
  "v_mfma_f32_16x16x32_bf16 v[10:13], a[240:243], v[30:33], v[10:13]\n"
  "v_and_b32 v30, v44, v5\n"
  "v_and_b32 v31, v44, v6\n"
  "v_and_b32 v32, v44, v7\n"
  "v_and_b32 v33, v44, v8\n"
  "v_mfma_f32_16x16x32_bf16 v[10:13], a[244:247], v[34:37], v[10:13]\n"
  "v_and_b32 v34, v45, v5\n"
  "v_and_b32 v35, v45, v6\n"
  "v_and_b32 v36, v45, v7\n"
  "v_and_b32 v37, v45, v8\n"
  "v_mfma_f32_16x16x32_bf16 v[10:13], a[248:251], v[30:33], v[10:13]\n"
  "s_nop 1\n"
  "v_mfma_f32_16x16x32_bf16 v[10:13], a[252:255], v[34:37], v[10:13]\n"
  "s_setpc_b64 s[58:59]\n"
  "L_LK8:\n"
  "s_add_u32 s57, s56, 65536\n"
  "buffer_load_dwordx4 v[184:187], v0, s[52:55], s57 offen offset:0 nt\n"
  "buffer_load_dwordx4 v[188:191], v0, s[52:55], s57 offen offset:1024 nt\n"
  "buffer_load_dwordx4 v[192:195], v0, s[52:55], s57 offen offset:2048 nt\n"
  "buffer_load_dwordx4 v[196:199], v0, s[52:55], s57 offen offset:3072 nt\n"
  "s_add_u32 s57, s56, 69632\n"
  "buffer_load_dwordx4 v[200:203], v0, s[52:55], s57 offen offset:0 nt\n"
  "buffer_load_dwordx4 v[204:207], v0, s[52:55], s57 offen offset:1024 nt\n"
  "buffer_load_dwordx4 v[208:211], v0, s[52:55], s57 offen offset:2048 nt\n"
  "buffer_load_dwordx4 v[212:215], v0, s[52:55], s57 offen offset:3072 nt\n"
  "s_add_u32 s57, s56, 73728\n"
  "buffer_load_dwordx4 v[216:219], v0, s[52:55], s57 offen offset:0 nt\n"
  "s_setpc_b64 s[58:59]\n"
  "L_LV8:\n"
  "s_add_u32 s57, s56, 65536\n"
  "buffer_load_dwordx4 v[220:223], v0, s[52:55], s57 offen offset:0 nt\n"
  "buffer_load_dwordx4 v[224:227], v0, s[52:55], s57 offen offset:1024 nt\n"
  "buffer_load_dwordx4 v[228:231], v0, s[52:55], s57 offen offset:2048 nt\n"
  "buffer_load_dwordx4 v[232:235], v0, s[52:55], s57 offen offset:3072 nt\n"
  "s_add_u32 s57, s56, 69632\n"
  "buffer_load_dwordx4 v[236:239], v0, s[52:55], s57 offen offset:0 nt\n"
  "buffer_load_dwordx4 v[240:243], v0, s[52:55], s57 offen offset:1024 nt\n"
  "buffer_load_dwordx4 v[244:247], v0, s[52:55], s57 offen offset:2048 nt\n"
  "buffer_load_dwordx4 v[248:251], v0, s[52:55], s57 offen offset:3072 nt\n"
  "s_add_u32 s57, s56, 73728\n"
  "buffer_load_dwordx4 v[252:255], v0, s[52:55], s57 offen offset:0 nt\n"
  "s_setpc_b64 s[58:59]\n"
  "L_KC8:\n"
  "v_dot2_f32_bf16 v22, v184, v18, 0\n"
  "v_dot2_f32_bf16 v22, v185, v19, v22\n"
  "v_dot2_f32_bf16 v22, v186, v20, v22\n"
  "v_dot2_f32_bf16 v22, v187, v21, v22\n"
  "v_dot2_f32_bf16 v23, v188, v18, 0\n"
  "v_dot2_f32_bf16 v23, v189, v19, v23\n"
  "v_dot2_f32_bf16 v23, v190, v20, v23\n"
  "v_dot2_f32_bf16 v23, v191, v21, v23\n"
  "v_dot2_f32_bf16 v24, v192, v18, 0\n"
  "v_dot2_f32_bf16 v24, v193, v19, v24\n"
  "v_dot2_f32_bf16 v24, v194, v20, v24\n"
  "v_dot2_f32_bf16 v24, v195, v21, v24\n"
  "v_dot2_f32_bf16 v25, v196, v18, 0\n"
  "v_dot2_f32_bf16 v25, v197, v19, v25\n"
  "v_dot2_f32_bf16 v25, v198, v20, v25\n"
  "v_dot2_f32_bf16 v25, v199, v21, v25\n"
  "v_dot2_f32_bf16 v26, v200, v18, 0\n"
  "v_dot2_f32_bf16 v26, v201, v19, v26\n"
  "v_dot2_f32_bf16 v26, v202, v20, v26\n"
  "v_dot2_f32_bf16 v26, v203, v21, v26\n"
  "v_dot2_f32_bf16 v27, v204, v18, 0\n"
  "v_dot2_f32_bf16 v27, v205, v19, v27\n"
  "v_dot2_f32_bf16 v27, v206, v20, v27\n"
  "v_dot2_f32_bf16 v27, v207, v21, v27\n"
  "v_dot2_f32_bf16 v28, v208, v18, 0\n"
  "v_dot2_f32_bf16 v28, v209, v19, v28\n"
  "v_dot2_f32_bf16 v28, v210, v20, v28\n"
  "v_dot2_f32_bf16 v28, v211, v21, v28\n"
  "v_dot2_f32_bf16 v29, v212, v18, 0\n"
  "v_dot2_f32_bf16 v29, v213, v19, v29\n"
  "v_dot2_f32_bf16 v29, v214, v20, v29\n"
  "v_dot2_f32_bf16 v29, v215, v21, v29\n"
  "s_nop 2\n"
  "v_permlane16_swap_b32 v22, v23\n"
  "v_permlane16_swap_b32 v24, v25\n"
  "v_permlane16_swap_b32 v26, v27\n"
  "v_permlane16_swap_b32 v28, v29\n"
  "s_nop 1\n"
  "v_add_f32 v22, v22, v23\n"
  "v_add_f32 v24, v24, v25\n"
  "v_add_f32 v26, v26, v27\n"
  "v_add_f32 v28, v28, v29\n"
  "s_nop 1\n"
  "v_add_f32_dpp v22, v22, v22 row_ror:8 row_mask:0xf bank_mask:0x3\n"
  "v_add_f32_dpp v26, v26, v26 row_ror:8 row_mask:0xf bank_mask:0x3\n"
  "v_add_f32_dpp v22, v24, v24 row_ror:8 row_mask:0xf bank_mask:0xc\n"
  "v_add_f32_dpp v26, v28, v28 row_ror:8 row_mask:0xf bank_mask:0xc\n"
  "s_nop 1\n"
  "v_add_f32_dpp v22, v22, v22 row_half_mirror row_mask:0xf bank_mask:0x5\n"
  "v_add_f32_dpp v22, v26, v26 row_half_mirror row_mask:0xf bank_mask:0xa\n"
  "s_nop 1\n"
  "v_add_f32_dpp v22, v22, v22 quad_perm:[1,0,3,2] row_mask:0xf bank_mask:0xf\n"
  "s_nop 1\n"
  "v_add_f32_dpp v22, v22, v22 quad_perm:[2,3,0,1] row_mask:0xf bank_mask:0xf\n"
  "ds_write_b32 v2, v22 offset:256\n"
  "v_dot2_f32_bf16 v23, v216, v18, 0\n"
  "v_dot2_f32_bf16 v23, v217, v19, v23\n"
  "v_dot2_f32_bf16 v23, v218, v20, v23\n"
  "v_dot2_f32_bf16 v23, v219, v21, v23\n"
  "s_nop 1\n"
  "s_nop 1\n"
  "v_add_f32_dpp v23, v23, v23 quad_perm:[1,0,3,2] row_mask:0xf bank_mask:0xf\n"
  "s_nop 1\n"
  "v_add_f32_dpp v23, v23, v23 quad_perm:[2,3,0,1] row_mask:0xf bank_mask:0xf\n"
  "s_nop 1\n"
  "v_add_f32_dpp v23, v23, v23 row_half_mirror row_mask:0xf bank_mask:0xf\n"
  "s_nop 1\n"
  "v_add_f32_dpp v23, v23, v23 row_mirror row_mask:0xf bank_mask:0xf\n"
  "v_mov_b32 v24, v23\n"
  "s_nop 1\n"
  "v_permlane16_swap_b32 v23, v24\n"
  "s_nop 1\n"
  "v_add_f32 v23, v23, v24\n"
  "ds_write_b32 v3, v23 offset:288\n"
  "s_setpc_b64 s[58:59]\n"
  "L_VC8:\n"
  "ds_read_b128 v[38:41], v3 offset:256\n"
  "ds_read_b128 v[42:45], v3 offset:272\n"
  "s_waitcnt lgkmcnt(0)\n"
  "v_and_b32 v30, v38, v5\n"
  "v_and_b32 v31, v38, v6\n"
  "v_and_b32 v32, v38, v7\n"
  "v_and_b32 v33, v38, v8\n"
  "v_and_b32 v34, v39, v5\n"
  "v_and_b32 v35, v39, v6\n"
  "v_and_b32 v36, v39, v7\n"
  "v_and_b32 v37, v39, v8\n"
  "v_mfma_f32_16x16x32_bf16 v[10:13], v[220:223], v[30:33], v[10:13]\n"
  "v_and_b32 v30, v40, v5\n"
  "v_and_b32 v31, v40, v6\n"
  "v_and_b32 v32, v40, v7\n"
  "v_and_b32 v33, v40, v8\n"
  "v_mfma_f32_16x16x32_bf16 v[10:13], v[224:227], v[34:37], v[10:13]\n"
  "v_and_b32 v34, v41, v5\n"
  "v_and_b32 v35, v41, v6\n"
  "v_and_b32 v36, v41, v7\n"
  "v_and_b32 v37, v41, v8\n"
  "v_mfma_f32_16x16x32_bf16 v[10:13], v[228:231], v[30:33], v[10:13]\n"
  "v_and_b32 v30, v42, v5\n"
  "v_and_b32 v31, v42, v6\n"
  "v_and_b32 v32, v42, v7\n"
  "v_and_b32 v33, v42, v8\n"
  "v_mfma_f32_16x16x32_bf16 v[10:13], v[232:235], v[34:37], v[10:13]\n"
  "v_and_b32 v34, v43, v5\n"
  "v_and_b32 v35, v43, v6\n"
  "v_and_b32 v36, v43, v7\n"
  "v_and_b32 v37, v43, v8\n"
  "v_mfma_f32_16x16x32_bf16 v[10:13], v[236:239], v[30:33], v[10:13]\n"
  "v_and_b32 v30, v44, v5\n"
  "v_and_b32 v31, v44, v6\n"
  "v_and_b32 v32, v44, v7\n"
  "v_and_b32 v33, v44, v8\n"
  "v_mfma_f32_16x16x32_bf16 v[10:13], v[240:243], v[34:37], v[10:13]\n"
  "v_and_b32 v34, v45, v5\n"
  "v_and_b32 v35, v45, v6\n"
  "v_and_b32 v36, v45, v7\n"
  "v_and_b32 v37, v45, v8\n"
  "v_mfma_f32_16x16x32_bf16 v[10:13], v[244:247], v[30:33], v[10:13]\n"
  "ds_read_b32 v38, v3 offset:288\n"
  "s_waitcnt lgkmcnt(0)\n"
  "v_and_b32 v30, v38, v5\n"
  "v_and_b32 v31, v38, v6\n"
  "v_and_b32 v32, v38, v7\n"
  "v_and_b32 v33, v38, v8\n"
  "v_mfma_f32_16x16x32_bf16 v[10:13], v[248:251], v[34:37], v[10:13]\n"
  "s_nop 1\n"
  "v_mfma_f32_16x16x32_bf16 v[10:13], v[252:255], v[30:33], v[10:13]\n"
  "s_setpc_b64 s[58:59]\n"
  "L_PACE:\n"
  "s_add_u32 s34, s34, 112\n"
  "L_PACE1:\n"
  "s_memrealtime s[2:3]\n"
  "s_waitcnt lgkmcnt(0)\n"
  "s_sub_u32 s2, s2, s34\n"
  "s_cmp_lt_i32 s2, 0\n"
  "s_cbranch_scc1 L_PACE1\n"
  "s_setpc_b64 s[58:59]\n"
  "L_QISS:\n"
  "L_QISS2:\n"
  "v_and_b32 v30, 0x1f0, v0\n"
  "v_lshlrev_b32 v30, 1, v30\n"
  "global_load_dwordx4 v[22:25], v30, s[66:67] sc1\n"
  "global_load_dwordx4 v[26:29], v30, s[66:67] offset:16 sc1\n"
  "s_setpc_b64 s[58:59]\n"
  "L_QCHK:\n"
  "s_mov_b32 s77, 0\n"
  "s_add_u32 s74, s91, 1\n"
  "L_QCHK1:\n"
  "v_xor_b32 v30, s74, v23\n"
  "v_xor_b32 v31, s74, v25\n"
  "v_xor_b32 v32, s74, v27\n"
  "v_xor_b32 v33, s74, v29\n"
  "v_or3_b32 v30, v30, v31, v32\n"
  "v_or_b32 v30, v30, v33\n"
  "v_cmp_ne_u32 vcc, 0, v30\n"
  "s_nop 4\n"
  "s_cbranch_vccz L_QOK\n"
  "s_add_u32 s77, s77, 1\n"
  "s_cmp_gt_u32 s77, 100000\n"
  "s_cbranch_scc1 L_QOK\n"
  "s_sleep 8\n"
  "v_and_b32 v30, 0x1f0, v0\n"
  "v_lshlrev_b32 v30, 1, v30\n"
  "global_load_dwordx4 v[22:25], v30, s[66:67] sc1\n"
  "global_load_dwordx4 v[26:29], v30, s[66:67] offset:16 sc1\n"
  "s_waitcnt vmcnt(0)\n"
  "s_branch L_QCHK1\n"
  "L_QOK:\n"
  "v_mov_b32 v18, v22\n"
  "v_mov_b32 v19, v24\n"
  "v_mov_b32 v20, v26\n"
  "v_mov_b32 v21, v28\n"
  "s_nop 1\n"
  "s_setpc_b64 s[58:59]\n"
  "L_SMX:\n"
  "v_lshrrev_b32 v25, 4, v0\n"
  "v_and_b32 v26, 15, v25\n"
  "v_add_u32 v26, 64, v26\n"
  "v_bfe_u32 v29, v25, 4, 1\n"
  "v_mul_u32_u24 v29, 320, v29\n"
  "v_lshl_add_u32 v27, v26, 2, v29\n"
  "v_add_u32 v27, s80, v27\n"
  "v_and_b32 v29, 32, v25\n"
  "v_lshl_add_u32 v26, v29, 2, v26\n"
  "s_waitcnt lgkmcnt(0)\n"
  "ds_read_b32 v22, v4\n"
  "ds_read_b32 v23, v4 offset:320\n"
  "ds_read_b32 v24, v27\n"
  "v_mov_b32 v30, 0xff800000\n"
  "s_waitcnt lgkmcnt(0)\n"
  "v_cvt_pk_bf16_f32 v22, v22, v22\n"
  "v_and_b32 v22, 0xffff0000, v22\n"
  "v_cvt_pk_bf16_f32 v23, v23, v23\n"
  "v_and_b32 v23, 0xffff0000, v23\n"
  "v_cvt_pk_bf16_f32 v24, v24, v24\n"
  "v_and_b32 v24, 0xffff0000, v24\n"
  "v_cmp_gt_i32 vcc, s63, v25\n"
  "s_nop 1\n"
  "v_cndmask_b32 v22, v30, v22, vcc\n"
  "v_cndmask_b32 v23, v30, v23, vcc\n"
  "v_cmp_gt_i32 vcc, s63, v26\n"
  "s_nop 1\n"
  "v_cndmask_b32 v24, v30, v24, vcc\n"
  "v_max3_f32 v28, v22, v23, v24\n"
  "s_nop 1\n"
  "v_max_f32_dpp v28, v28, v28 quad_perm:[1,0,3,2] row_mask:0xf bank_mask:0xf\n"
  "s_nop 1\n"
  "v_max_f32_dpp v28, v28, v28 quad_perm:[2,3,0,1] row_mask:0xf bank_mask:0xf\n"
  "s_nop 1\n"
  "v_max_f32_dpp v28, v28, v28 row_half_mirror row_mask:0xf bank_mask:0xf\n"
  "s_nop 1\n"
  "v_max_f32_dpp v28, v28, v28 row_mirror row_mask:0xf bank_mask:0xf\n"
  "v_mov_b32 v29, v28\n"
  "s_nop 1\n"
  "v_permlane16_swap_b32 v28, v29\n"
  "s_nop 1\n"
  "v_max_f32 v28, v28, v29\n"
  "v_mov_b32 v29, v28\n"
  "s_nop 1\n"
  "v_permlane32_swap_b32 v28, v29\n"
  "s_nop 1\n"
  "v_max_f32 v28, v28, v29\n"
  "v_max_f32 v28, v28, v9\n"
  "v_sub_f32 v31, v9, v28\n"
  "v_mul_f32 v31, s78, v31\n"
  "v_exp_f32 v31, v31\n"
  "v_mov_b32 v9, v28\n"
  "v_sub_f32 v22, v22, v28\n"
  "v_mul_f32 v22, s78, v22\n"
  "v_exp_f32 v22, v22\n"
  "v_sub_f32 v23, v23, v28\n"
  "v_mul_f32 v23, s78, v23\n"
  "v_exp_f32 v23, v23\n"
  "v_sub_f32 v24, v24, v28\n"
  "v_mul_f32 v24, s78, v24\n"
  "v_exp_f32 v24, v24\n"
  "v_mul_f32 v46, v46, v31\n"
  "v_mul_f32 v10, v10, v31\n"
  "v_mul_f32 v11, v11, v31\n"
  "v_mul_f32 v12, v12, v31\n"
  "v_mul_f32 v13, v13, v31\n"
  "v_cvt_pk_bf16_f32 v22, v22, v22\n"
  "v_and_b32 v29, 0xffff0000, v22\n"
  "v_add_f32 v46, v46, v29\n"
  "v_cvt_pk_bf16_f32 v23, v23, v23\n"
  "v_and_b32 v29, 0xffff0000, v23\n"
  "v_add_f32 v46, v46, v29\n"
  "v_cvt_pk_bf16_f32 v24, v24, v24\n"
  "v_and_b32 v29, 0xffff0000, v24\n"
  "v_add_f32 v46, v46, v29\n"
  "ds_write_b32 v4, v22\n"
  "ds_write_b32 v4, v23 offset:320\n"
  "s_mov_b32 exec_hi, 0\n"
  "ds_write_b32 v27, v24\n"
  "s_mov_b32 exec_hi, -1\n"
  "s_waitcnt lgkmcnt(0)\n"
  "s_setpc_b64 s[58:59]\n"
  "L_STREAM:\n"
  "s_mov_b64 s[82:83], s[58:59]\n"
  "s_mov_b32 s65, 0\n"
  "v_mov_b32 v38, v2\n"
  "L_SK:\n"
  "s_cmp_lg_u32 s65, 7\n"
  "s_cbranch_scc1 L_SK1\n"
  "s_mov_b64 s[52:53], s[86:87]\n"
  "s_mov_b32 s56, s61\n"
  "L_SK1:\n"
  "s_add_u32 s57, s56, 4096\n"
  "s_waitcnt vmcnt(7)\n"
  "v_dot2_f32_bf16 v22, v48, v18, 0\n"
  "v_dot2_f32_bf16 v22, v49, v19, v22\n"
  "v_dot2_f32_bf16 v22, v50, v20, v22\n"
  "v_dot2_f32_bf16 v22, v51, v21, v22\n"
  "buffer_load_dwordx4 v[48:51], v0, s[52:55], s56 offen offset:0 nt\n"
  "s_waitcnt vmcnt(7)\n"
  "v_dot2_f32_bf16 v23, v52, v18, 0\n"
  "v_dot2_f32_bf16 v23, v53, v19, v23\n"
  "v_dot2_f32_bf16 v23, v54, v20, v23\n"
  "v_dot2_f32_bf16 v23, v55, v21, v23\n"
  "buffer_load_dwordx4 v[52:55], v0, s[52:55], s56 offen offset:1024 nt\n"
  "s_waitcnt vmcnt(7)\n"
  "v_dot2_f32_bf16 v24, v56, v18, 0\n"
  "v_dot2_f32_bf16 v24, v57, v19, v24\n"
  "v_dot2_f32_bf16 v24, v58, v20, v24\n"
  "v_dot2_f32_bf16 v24, v59, v21, v24\n"
  "buffer_load_dwordx4 v[56:59], v0, s[52:55], s56 offen offset:2048 nt\n"
  "s_waitcnt vmcnt(7)\n"
  "v_dot2_f32_bf16 v25, v60, v18, 0\n"
  "v_dot2_f32_bf16 v25, v61, v19, v25\n"
  "v_dot2_f32_bf16 v25, v62, v20, v25\n"
  "v_dot2_f32_bf16 v25, v63, v21, v25\n"
  "buffer_load_dwordx4 v[60:63], v0, s[52:55], s56 offen offset:3072 nt\n"
  "s_waitcnt vmcnt(7)\n"
  "v_dot2_f32_bf16 v26, v64, v18, 0\n"
  "v_dot2_f32_bf16 v26, v65, v19, v26\n"
  "v_dot2_f32_bf16 v26, v66, v20, v26\n"
  "v_dot2_f32_bf16 v26, v67, v21, v26\n"
  "buffer_load_dwordx4 v[64:67], v0, s[52:55], s57 offen offset:0 nt\n"
  "s_waitcnt vmcnt(7)\n"
  "v_dot2_f32_bf16 v27, v68, v18, 0\n"
  "v_dot2_f32_bf16 v27, v69, v19, v27\n"
  "v_dot2_f32_bf16 v27, v70, v20, v27\n"
  "v_dot2_f32_bf16 v27, v71, v21, v27\n"
  "buffer_load_dwordx4 v[68:71], v0, s[52:55], s57 offen offset:1024 nt\n"
  "s_waitcnt vmcnt(7)\n"
  "v_dot2_f32_bf16 v28, v72, v18, 0\n"
  "v_dot2_f32_bf16 v28, v73, v19, v28\n"
  "v_dot2_f32_bf16 v28, v74, v20, v28\n"
  "v_dot2_f32_bf16 v28, v75, v21, v28\n"
  "buffer_load_dwordx4 v[72:75], v0, s[52:55], s57 offen offset:2048 nt\n"
  "s_waitcnt vmcnt(7)\n"
  "v_dot2_f32_bf16 v29, v76, v18, 0\n"
  "v_dot2_f32_bf16 v29, v77, v19, v29\n"
  "v_dot2_f32_bf16 v29, v78, v20, v29\n"
  "v_dot2_f32_bf16 v29, v79, v21, v29\n"
  "buffer_load_dwordx4 v[76:79], v0, s[52:55], s57 offen offset:3072 nt\n"
  "s_nop 2\n"
  "v_permlane16_swap_b32 v22, v23\n"
  "v_permlane16_swap_b32 v24, v25\n"
  "v_permlane16_swap_b32 v26, v27\n"
  "v_permlane16_swap_b32 v28, v29\n"
  "s_nop 1\n"
  "v_add_f32 v22, v22, v23\n"
  "v_add_f32 v24, v24, v25\n"
  "v_add_f32 v26, v26, v27\n"
  "v_add_f32 v28, v28, v29\n"
  "s_nop 1\n"
  "v_add_f32_dpp v22, v22, v22 row_ror:8 row_mask:0xf bank_mask:0x3\n"
  "v_add_f32_dpp v26, v26, v26 row_ror:8 row_mask:0xf bank_mask:0x3\n"
  "v_add_f32_dpp v22, v24, v24 row_ror:8 row_mask:0xf bank_mask:0xc\n"
  "v_add_f32_dpp v26, v28, v28 row_ror:8 row_mask:0xf bank_mask:0xc\n"
  "s_nop 1\n"
  "v_add_f32_dpp v22, v22, v22 row_half_mirror row_mask:0xf bank_mask:0x5\n"
  "v_add_f32_dpp v22, v26, v26 row_half_mirror row_mask:0xf bank_mask:0xa\n"
  "s_nop 1\n"
  "v_add_f32_dpp v22, v22, v22 quad_perm:[1,0,3,2] row_mask:0xf bank_mask:0xf\n"
  "s_nop 1\n"
  "v_add_f32_dpp v22, v22, v22 quad_perm:[2,3,0,1] row_mask:0xf bank_mask:0xf\n"
  "ds_write_b32 v38, v22\n"
  "v_add_u32 v38, 32, v38\n"
  "s_add_u32 s56, s56, 8192\n"
  "s_add_u32 s65, s65, 1\n"
  "s_cmp_lt_u32 s65, 8\n"
  "s_cbranch_scc1 L_SK\n"
  "s_mov_b32 s63, s62\n"
  "s_call_b64 s[58:59], L_SMX\n"
  "s_call_b64 s[58:59], L_QISS\n"
  "s_mov_b32 s65, 0\n"
  "v_mov_b32 v18, v3\n"
  "L_SV:\n"
  "ds_read_b128 v[38:41], v18\n"
  "ds_read_b128 v[42:45], v18 offset:16\n"
  "s_cmp_lg_u32 s65, 7\n"
  "s_cbranch_scc1 L_SV1\n"
  "s_mov_b32 s54, 0\n"
  "L_SV1:\n"
  "s_add_u32 s57, s56, 4096\n"
  "v_add_u32 v18, 32, v18\n"
  "s_waitcnt lgkmcnt(0)\n"
  "v_and_b32 v30, v38, v5\n"
  "v_and_b32 v31, v38, v6\n"
  "v_and_b32 v32, v38, v7\n"
  "v_and_b32 v33, v38, v8\n"
  "v_and_b32 v34, v39, v5\n"
  "v_and_b32 v35, v39, v6\n"
  "v_and_b32 v36, v39, v7\n"
  "v_and_b32 v37, v39, v8\n"
  "s_waitcnt vmcnt(7)\n"
  "v_mfma_f32_16x16x32_bf16 v[10:13], v[48:51], v[30:33], v[10:13]\n"
  "buffer_load_dwordx4 v[48:51], v0, s[52:55], s56 offen offset:0 nt\n"
  "v_and_b32 v30, v40, v5\n"
  "v_and_b32 v31, v40, v6\n"
  "v_and_b32 v32, v40, v7\n"
  "v_and_b32 v33, v40, v8\n"
  "s_waitcnt vmcnt(7)\n"
  "v_mfma_f32_16x16x32_bf16 v[10:13], v[52:55], v[34:37], v[10:13]\n"
  "buffer_load_dwordx4 v[52:55], v0, s[52:55], s56 offen offset:1024 nt\n"
  "v_and_b32 v34, v41, v5\n"
  "v_and_b32 v35, v41, v6\n"
  "v_and_b32 v36, v41, v7\n"
  "v_and_b32 v37, v41, v8\n"
  "s_waitcnt vmcnt(7)\n"
  "v_mfma_f32_16x16x32_bf16 v[10:13], v[56:59], v[30:33], v[10:13]\n"
  "buffer_load_dwordx4 v[56:59], v0, s[52:55], s56 offen offset:2048 nt\n"
  "v_and_b32 v30, v42, v5\n"
  "v_and_b32 v31, v42, v6\n"
  "v_and_b32 v32, v42, v7\n"
  "v_and_b32 v33, v42, v8\n"
  "s_waitcnt vmcnt(7)\n"
  "v_mfma_f32_16x16x32_bf16 v[10:13], v[60:63], v[34:37], v[10:13]\n"
  "buffer_load_dwordx4 v[60:63], v0, s[52:55], s56 offen offset:3072 nt\n"
  "v_and_b32 v34, v43, v5\n"
  "v_and_b32 v35, v43, v6\n"
  "v_and_b32 v36, v43, v7\n"
  "v_and_b32 v37, v43, v8\n"
  "s_waitcnt vmcnt(7)\n"
  "v_mfma_f32_16x16x32_bf16 v[10:13], v[64:67], v[30:33], v[10:13]\n"
  "buffer_load_dwordx4 v[64:67], v0, s[52:55], s57 offen offset:0 nt\n"
  "v_and_b32 v30, v44, v5\n"
  "v_and_b32 v31, v44, v6\n"
  "v_and_b32 v32, v44, v7\n"
  "v_and_b32 v33, v44, v8\n"
  "s_waitcnt vmcnt(7)\n"
  "v_mfma_f32_16x16x32_bf16 v[10:13], v[68:71], v[34:37], v[10:13]\n"
  "buffer_load_dwordx4 v[68:71], v0, s[52:55], s57 offen offset:1024 nt\n"
  "v_and_b32 v34, v45, v5\n"
  "v_and_b32 v35, v45, v6\n"
  "v_and_b32 v36, v45, v7\n"
  "v_and_b32 v37, v45, v8\n"
  "s_waitcnt vmcnt(7)\n"
  "v_mfma_f32_16x16x32_bf16 v[10:13], v[72:75], v[30:33], v[10:13]\n"
  "buffer_load_dwordx4 v[72:75], v0, s[52:55], s57 offen offset:2048 nt\n"
  "s_nop 1\n"
  "s_waitcnt vmcnt(7)\n"
  "v_mfma_f32_16x16x32_bf16 v[10:13], v[76:79], v[34:37], v[10:13]\n"
  "buffer_load_dwordx4 v[76:79], v0, s[52:55], s57 offen offset:3072 nt\n"
  "s_add_u32 s56, s56, 8192\n"
  "s_add_u32 s65, s65, 1\n"
  "s_cmp_lt_u32 s65, 8\n"
  "s_cbranch_scc1 L_SV\n"
  "s_setpc_b64 s[82:83]\n"
  "L_PUB:\n"
  "s_mul_i32 s57, s76, 1040\n"
  "s_add_u32 s57, s57, s90\n"
  "v_add_u32 v30, s57, v0\n"
  "v_mov_b32 v31, s57\n"
  "s_nop 1\n"
  "v_add_f32_dpp v46, v46, v46 quad_perm:[1,0,3,2] row_mask:0xf bank_mask:0xf\n"
  "s_nop 1\n"
  "v_add_f32_dpp v46, v46, v46 quad_perm:[2,3,0,1] row_mask:0xf bank_mask:0xf\n"
  "s_nop 1\n"
  "v_add_f32_dpp v46, v46, v46 row_half_mirror row_mask:0xf bank_mask:0xf\n"
  "s_nop 1\n"
  "v_add_f32_dpp v46, v46, v46 row_mirror row_mask:0xf bank_mask:0xf\n"
  "v_mov_b32 v32, v46\n"
  "s_nop 1\n"
  "v_permlane16_swap_b32 v46, v32\n"
  "s_nop 1\n"
  "v_add_f32 v46, v46, v32\n"
  "v_mov_b32 v32, v46\n"
  "s_nop 1\n"
  "v_permlane32_swap_b32 v46, v32\n"
  "s_nop 1\n"
  "v_add_f32 v46, v46, v32\n"
  "v_mov_b32 v32, v9\n"
  "v_mov_b32 v33, v46\n"
  "ds_write_b128 v30, v[10:13]\n"
  "ds_write_b64 v31, v[32:33] offset:1024\n"
  "s_lshl_b32 s57, s76, 8\n"
  "s_add_u32 s57, s57, s90\n"
  "v_lshrrev_b32 v10, 2, v0\n"
  "v_add_u32 v10, s57, v10\n"
  "v_mov_b32 v11, s90\n"
  "s_waitcnt lgkmcnt(0)\n"
  "s_barrier\n"
  "ds_read_b32 v30, v10 offset:0\n"
  "ds_read_b32 v31, v10 offset:1040\n"
  "ds_read_b32 v32, v10 offset:2080\n"
  "ds_read_b32 v33, v10 offset:3120\n"
  "ds_read_b64 v[34:35], v11 offset:1024\n"
  "ds_read_b64 v[36:37], v11 offset:2064\n"
  "ds_read_b64 v[38:39], v11 offset:3104\n"
  "ds_read_b64 v[40:41], v11 offset:4144\n"
  "s_waitcnt lgkmcnt(0)\n"
  "s_barrier\n"
  "v_max3_f32 v42, v34, v36, v38\n"
  "v_max_f32 v42, v42, v40\n"
  "v_sub_f32 v34, v34, v42\n"
  "v_mul_f32 v34, s78, v34\n"
  "v_exp_f32 v34, v34\n"
  "v_sub_f32 v36, v36, v42\n"
  "v_mul_f32 v36, s78, v36\n"
  "v_exp_f32 v36, v36\n"
  "v_sub_f32 v38, v38, v42\n"
  "v_mul_f32 v38, s78, v38\n"
  "v_exp_f32 v38, v38\n"
  "v_sub_f32 v40, v40, v42\n"
  "v_mul_f32 v40, s78, v40\n"
  "v_exp_f32 v40, v40\n"
  "s_nop 0\n"
  "v_mul_f32 v12, v30, v34\n"
  "v_mul_f32 v44, v35, v34\n"
  "v_fmac_f32 v12, v31, v36\n"
  "v_fmac_f32 v44, v37, v36\n"
  "v_fmac_f32 v12, v32, v38\n"
  "v_fmac_f32 v44, v39, v38\n"
  "v_fmac_f32 v12, v33, v40\n"
  "v_fmac_f32 v44, v41, v40\n"
  "v_mov_b32 v13, s74\n"
  "v_mov_b32 v43, s74\n"
  "v_mov_b32 v45, s74\n"
  "s_lshl_b32 s57, s76, 9\n"
  "v_lshrrev_b32 v10, 1, v0\n"
  "v_add_u32 v10, s57, v10\n"
  "global_store_dwordx2 v10, v[12:13], s[72:73] sc1\n"
  "s_cmp_lg_u32 s76, 0\n"
  "s_cbranch_scc1 L_PUB1\n"
  "s_mov_b64 exec, 1\n"
  "global_store_dwordx2 v10, v[42:43], s[72:73] offset:2048 sc1\n"
  "global_store_dwordx2 v10, v[44:45], s[72:73] offset:2056 sc1\n"
  "s_mov_b64 exec, -1\n"
  "L_PUB1:\n"
  "s_nop 1\n"
  "s_setpc_b64 s[58:59]\n"
  "L_END:\n"
  "s_waitcnt vmcnt(0)\n"
      "s_endpgm\n"
      :
      : "s"(kb), "s"(vb), "s"(qp), "s"(pp), "s"(blk0), "s"(w), "s"(lbase), "s"(wsp + WS_E)
      : "v255", "a255", "s36", "s37", "s38", "s39", "s40", "s41", "s42", "s43", "s44", "s45", "s46", "s47", "s48", "s49", "s50", "s51", "s52", "s53", "s54", "s55", "s56", "s57", "s58", "s59", "s60", "s61", "s62", "s63", "s64", "s65", "s66", "s67", "s68", "s69", "s70", "s71", "s72", "s73", "s74", "s75", "s76", "s77", "s78", "s79", "s80", "s81", "s82", "s83", "s84", "s85", "s86", "s87", "s88", "s89", "s90", "s91", "vcc", "scc", "memory");
    return;
  }
  const int c = bid - NATT;
  const rsrc_t ws = __builtin_amdgcn_make_buffer_rsrc(wsp, 0, WS_END, 0x20000);
  const u32x4 rq = mkdesc(wq, D * 4096 * 2), ro = mkdesc(wo, D * 4096 * 2);
  float* red = (float*)(lds + C_RED);
  float* red2 = red + 4 * D;
  unsigned* vec = (unsigned*)(lds + C_VEC + w * 2176);
#define FRESH() do { asm volatile("" : "+v"(tid)); lane = tid & 63; } while (0)
#define vr (vec + (lane >> 3) * 68)
#define xs ((float*)(lds + C_X) + lane)
#define msk mkmask(lane)
  TSC(0);
  unsigned ep;
  asm volatile("s_load_dword %0, %1, 0x0" : "=s"(ep) : "s"(wsp + WS_E) : "memory");
  float x0[7], x1[7];
#pragma unroll
  for (int i = 0; i < 7; i++) { x0[i] = bf2f(xin[lane + 64 * i]); x1[i] = bf2f(xin[D + lane + 64 * i]); }
  u32x4 Wg[47];
  load_wq<0, 56, true>(rq, c, w, lane);
  asm volatile("s_waitcnt lgkmcnt(0)" : "+s"(ep));
  for (int i = tid; i < 15 * 260; i += 256) {
    gstore(ws, i * 8, WS_P + c * 15 * 2080, 0u, ep);
    gstore(ws, i * 8, WS_P + (16 + c) * 15 * 2080, 0u, ep);
  }
  gstore(ws, (tid >> 7) * 16384 + (tid & 127) * 8, WS_Q + 32768 + c * 1024, 0u, ep);
  gstore(ws, tid * 8, WS_Y + c * 2048, ep & 0xffu, (ep >> 8) & 0xffu); gstore(ws, tid * 8, WS_Z + c * 2048, ep & 0xffu, (ep >> 8) & 0xffu);
  ln_wave(x0, vec, lane);
  VFENCE();
  q_proj(vr, msk, red, ws, WS_Q + c * 1024, ep + 1, tid, w);
  if (tid == 0) gstore(ws, 0, WS_F + c * 8, 0u, ep + 1);
  TSC(1);
  ln_wave(x1, vec, lane);
  q_proj(vr, msk, red2, ws, WS_Q + 16384 + c * 1024, ep + 1, tid, w);
  TSC(2);
  load_wo(ro, c, w, lane);
#pragma unroll
  for (int i = 0; i < 7; i++) { xs[64 * i] = x0[i]; xs[D + 64 * i] = x1[i]; }
  {
    const rsrc_t rg = __builtin_amdgcn_make_buffer_rsrc((void*)wg, 0, D * FF * 2, 0x20000);
    const rsrc_t ru = __builtin_amdgcn_make_buffer_rsrc((void*)wu, 0, D * FF * 2, 0x20000);
    const int voff = (lane >> 4) * 7168 + min(lane & 15, 13) * 16;
#pragma unroll
    for (int i = 0; i < 8; i++) aload<true>(56 + i, mkdesc((i & 1) ? wu : wg, D * FF * 2), voff, (112 * w + 8 * (i >> 2)) * 3584 + c * 224, ((i >> 1) & 1) * 3584);
#pragma unroll
    for (int i = 8; i < 47; i++) Wg[i] = wload((i & 1) ? ru : rg, voff + ((i >> 1) & 1) * 3584, (112 * w + 8 * (i >> 2)) * 3584 + c * 224);
#pragma unroll
    for (int i = 47; i < 56; i++)
      dma16((const char*)((i & 1) ? wu : wg) + voff + ((i >> 1) & 1) * 3584 + (112 * w + 8 * (i >> 2)) * 3584 + c * 224, lds + C_GU + w * 9216 + (i - 47) * 1024);
#pragma unroll
    for (int i = 0; i < 25; i++)
      if (i < 24 || w < 2) dma16((const char*)wd + c * 100352 + (i * 4 + w) * 1024 + lane * 16, lds + C_WD + (i * 4 + w) * 1024);
    for (int spin = 0; spin < SPIN_CAP; spin++) {
      MFENCE();
      if (__all(gload(ws, (lane & 15) * 8, WS_F)[1] == ep + 1)) break;
    }
    VFENCE();
    __syncthreads();
  }
#pragma unroll 1
  for (int round = 0; round < 4; round++) {
    const unsigned tag = ep + round + 1;
    const int b = round & 1;
    float x[7];
    TSC(3 + round * 8);
    FRESH();
    const int pin = WS_P + ((round == 2 ? 16 : 0) + c) * 15 * 2080;
    const int pj = lane < 15 ? lane : 0;
    for (int spin = 0; spin < SPIN_CAP; spin++) {
      MFENCE();
      const u32x2 g = gload(ws, pj * 2080 + 2056, pin);
      if (__any(g[1] == tag)) break;
      __builtin_amdgcn_s_sleep(16);
    }
    TSC(4 + round * 8);
    float ov[15], mj, lj;
    for (int spin = 0; spin < SPIN_CAP; spin++) {
      u32x2 g[15];
      MFENCE();
#pragma unroll
      for (int p = 0; p < 15; p++) g[p] = gload(ws, tid * 8, pin + p * 2080);
      const u32x2 gm = gload(ws, pj * 2080 + 2048, pin), gl = gload(ws, pj * 2080 + 2056, pin);
      bool ok = gm[1] == tag && gl[1] == tag;
#pragma unroll
      for (int p = 0; p < 15; p++) { ok &= g[p][1] == tag; ov[p] = __uint_as_float(g[p][0]); }
      mj = __uint_as_float(gm[0]); lj = __uint_as_float(gl[0]);
      if (__all(ok)) break;
    }
    TSC(5 + round * 8);
    FRESH();
    float M = mj;
    M = fmaxf(M, dppf<0xB1>(M)); M = fmaxf(M, dppf<0x4E>(M)); M = fmaxf(M, dppf<0x141>(M)); M = fmaxf(M, dppf<0x140>(M));
    const float sc = __builtin_amdgcn_exp2f((mj - M) * 0.09016844005556021f);
    float Ls = lane < 15 ? sc * lj : 0.f;
    Ls += dppf<0xB1>(Ls); Ls += dppf<0x4E>(Ls); Ls += dppf<0x141>(Ls); Ls += dppf<0x140>(Ls);
    float o = 0.f;
#pragma unroll
    for (int p = 0; p < 15; p++) o = fmaf(__builtin_bit_cast(float, __builtin_amdgcn_readlane(__builtin_bit_cast(int, sc), p)), ov[p], o);
    o *= __builtin_amdgcn_rcpf(__builtin_bit_cast(float, __builtin_amdgcn_readfirstlane(__builtin_bit_cast(int, Ls))));
    vec[((lane >> 2) & 7) * 68 + (lane >> 5) * 4 + (lane & 3)] = dup(o);
    LFENCE();
    VFENCE();
    asm volatile("" ::: "a0", "a1", "a2", "a3", "a4", "a5", "a6", "a7", "a8", "a9", "a10", "a11", "a12", "a13", "a14", "a15", "a16", "a17", "a18", "a19", "a20", "a21", "a22", "a23", "a24", "a25", "a26", "a27", "a28", "a29", "a30", "a31", "a32", "a33", "a34", "a35", "a36", "a37", "a38", "a39", "a40", "a41", "a42", "a43", "a44", "a45", "a46", "a47", "a48", "a49", "a50", "a51", "a52", "a53", "a54", "a55", "a56", "a57", "a58", "a59", "a60", "a61", "a62", "a63", "a64", "a65", "a66", "a67", "a68", "a69", "a70", "a71", "a72", "a73", "a74", "a75", "a76", "a77", "a78", "a79", "a80", "a81", "a82", "a83", "a84", "a85", "a86", "a87", "a88", "a89", "a90", "a91", "a92", "a93", "a94", "a95", "a96", "a97", "a98", "a99", "a100", "a101", "a102", "a103", "a104", "a105", "a106", "a107", "a108", "a109", "a110", "a111", "a112", "a113", "a114", "a115", "a116", "a117", "a118", "a119", "a120", "a121", "a122", "a123", "a124", "a125", "a126", "a127", "a128", "a129", "a130", "a131", "a132", "a133", "a134", "a135", "a136", "a137", "a138", "a139", "a140", "a141", "a142", "a143", "a144", "a145", "a146", "a147", "a148", "a149", "a150", "a151", "a152", "a153", "a154", "a155", "a156", "a157", "a158", "a159", "a160", "a161", "a162", "a163", "a164", "a165", "a166", "a167", "a168", "a169", "a170", "a171", "a172", "a173", "a174", "a175", "a176", "a177", "a178", "a179", "a180", "a181", "a182", "a183", "a184", "a185", "a186", "a187", "a188", "a189", "a190", "a191", "a192", "a193", "a194", "a195", "a196", "a197", "a198", "a199", "a200", "a201", "a202", "a203", "a204", "a205", "a206", "a207", "a208", "a209", "a210", "a211", "a212", "a213", "a214", "a215", "a216", "a217", "a218", "a219", "a220", "a221", "a222", "a223", "a224", "a225", "a226", "a227", "a228", "a229", "a230", "a231", "a232", "a233", "a234", "a235", "a236", "a237", "a238", "a239", "a240", "a241", "a242", "a243", "a244", "a245", "a246", "a247", "a248", "a249", "a250", "a251", "a252", "a253", "a254", "a255");
    f32x4 acc[7], acc4[4];
    unsigned xv[14];
#pragma unroll
    for (int t = 0; t < 8; t++) xv[t] = vr[t];
    mm_begin(acc);
#pragma unroll
    for (int t = 0; t < 8; t++) {
      const u32x4 bb = bop(msk, xv[t]);
#pragma unroll
      for (int i = 0; i < 7; i++) mma(t * 7 + i, bb, acc[i]);
    }
    mm_end(acc);
    reduce_publish(acc, red, ws, WS_Y + c * 2048, tag, tid, w, lane);
    if (round < 2) load_wq<0, PA, true>(rq, c, w, lane);
    TSC(6 + round * 8);
    FRESH();
    gather_add<PA, PB>(ws, WS_Y, tag, red2, xs + b * D, x, w, lane, round < 2, rq, c);
    TSC(7 + round * 8);
    FRESH();
    u32x4 A[9];
#pragma unroll
    for (int i = 0; i < 7; i++) xs[b * D + 64 * i] = x[i];
    ln_wave(x, vec, lane);
#pragma unroll
    for (int t = 0; t < 14; t++) xv[t] = vr[14 * w + t];
    mm_begin(acc4);
#pragma unroll
    for (int t = 0; t < 14; t++) {
      const u32x4 bb = bop(msk, xv[t]);
      if (round < 2) aload<true>(PB + t, rq, (lane >> 4) * 16384 + (lane & 15) * 16, (112 * w + 8 * ((PB + t) >> 2) + (((PB + t) >> 1) & 1)) * 8192 + c * 512, ((PB + t) & 1) * 256);
      if (t == 8 || t == 10) {
#pragma unroll
        for (int i = (t == 8 ? 0 : 5); i < (t == 8 ? 5 : 9); i++) A[i] = *(const u32x4*)(lds + C_GU + w * 9216 + i * 1024 + lane * 16);
      }
#pragma unroll
      for (int i = 0; i < 4; i++) {
        if (t < 2) mma(56 + t * 4 + i, bb, acc4[i]);
        else if (t * 4 + i < 47) mmv(Wg[t * 4 + i < 47 ? t * 4 + i : 0], bb, acc4[i]);
        else mmv(A[t * 4 + i < 47 ? 0 : t * 4 + i - 47], bb, acc4[i]);
      }
    }
    mm_end(acc4);
    put2(red + w * 256, acc4[0], acc4[2], lane);
    put2(red + w * 256 + 128, acc4[1], acc4[3], lane);
#pragma unroll
    for (int n = 0; n < 6; n++) A[n] = down_tile(lds + C_WD, n, w, lane);
    __syncthreads();
    if (lane < 32) {
      const int row = 8 * w + 32 * (lane >> 3) + (lane & 7);
      const float g = bfr((red[row] + red[256 + row]) + (red[512 + row] + red[768 + row]));
      const float u = bfr((red[128 + row] + red[384 + row]) + (red[640 + row] + red[896 + row]));
      vec[(lane & 7) * 68 + (lane >> 3)] = row < 112 ? dup(bfr(__builtin_amdgcn_rcpf(1.0f + __expf(-g))) * u) : 0u;
    }
    LFENCE();
#pragma unroll
    for (int t = 0; t < 4; t++) xv[t] = vr[t];
    mm_begin(acc);
#pragma unroll
    for (int t = 0; t < 4; t++) {
      const u32x4 bb = bop(msk, xv[t]);
#pragma unroll
      for (int i = 0; i < 7; i++) {
        const int n = t * 7 + i;
        mmv(A[n % 6], bb, acc[i]);
        if (n < 22) A[n % 6] = down_tile(lds + C_WD, n + 6, w, lane);
      }
    }
    mm_end(acc);
    reduce_publish(acc, red2, ws, WS_Z + c * 2048, tag, tid, w, lane);
    if (round < 2) load_wq<PB + 14, PC, true>(rq, c, w, lane);
    TSC(8 + round * 8);
    FRESH();
    if (round < 2) {
      gather_add<PC, 56>(ws, WS_Z, tag, red, xs + b * D, x, w, lane, true, rq, c);
      TSC(9 + round * 8);
      FRESH();
#pragma unroll
      for (int i = 0; i < 7; i++) xs[b * D + 64 * i] = x[i];
      ln_wave(x, vec, lane);
      q_proj(vr, msk, red2, ws, WS_Q + (2 + b) * 16384 + c * 1024, ep + 1, tid, w);
      FRESH();
      load_wo(ro, c, w, lane);
    } else {
      const int g0 = 16 * c + (lane & 3);
      float y[8];
      for (int spin = 0; spin < SPIN_CAP; spin++) {
        MFENCE();
        bool ok = true;
#pragma unroll
        for (int i = 0; i < 4; i++) {
          const u32x2 g = gload(ws, (lane >> 2) * 2048 + (g0 + 4 * i) * 8, WS_Z);
          ok &= tag_ok(g, tag); y[2 * i] = pk_val(g[0]); y[2 * i + 1] = pk_val(g[1]);
        }
        if (__all(ok)) break;
      }
      TSC(9 + round * 8);
      if (w == 0) {
#pragma unroll
        for (int i = 0; i < 8; i++) {
          float v = y[i];
          v += dppf<0x124>(v); v += dppf<0x128>(v);
          auto s = __builtin_amdgcn_permlane16_swap(__float_as_uint(v), __float_as_uint(v), false, false);
          v = __uint_as_float(s[0]) + __uint_as_float(s[1]);
          auto u = __builtin_amdgcn_permlane32_swap(__float_as_uint(v), __float_as_uint(v), false, false);
          y[i] = __uint_as_float(u[0]) + __uint_as_float(u[1]);
        }
        float v = y[0];
#pragma unroll
        for (int i = 1; i < 8; i++) v = (lane >> 2) == i ? y[i] : v;
        const int n = g0 + 4 * (lane >> 3) + 256 * ((lane >> 2) & 1), col = b * D + n;
        if (lane < 32 && n < D) out[col] = bfbits(((const float*)(lds + C_X))[col] + bfr(v));
        if (round == 3 && c == 0 && lane == 0) gstore(ws, 0, WS_E, ep + 8, 0u);
      }
      __syncthreads();
    }
    TSC(10 + round * 8);
  }
}

extern "C" void kernel_launch(void* const* d_in, const int* in_sizes, int n_in, void* d_out, int out_size, void* d_ws,
                              size_t ws_size, hipStream_t stream) {
  (void)in_sizes; (void)n_in; (void)out_size; (void)ws_size;
  fused_decode<<<256, 256, 0, stream>>>((const unsigned short*)d_in[0], (const char*)d_in[1], (const char*)d_in[2],
                                        (const unsigned short*)d_in[3], (const unsigned short*)d_in[4], (const unsigned short*)d_in[6],
                                        (const unsigned short*)d_in[7], (const unsigned short*)d_in[8], (unsigned short*)d_out, (char*)d_ws);
}
```
